```python
import torch

def _softmax(x, axis=-1):
    return torch.softmax(x, dim=axis)
def _gelu(x):
    return torch.nn.functional.gelu(x, approximate="tanh")
def _silu(x):
    return torch.nn.functional.silu(x)
def _sigmoid(x):
    return torch.sigmoid(x)
def _layernorm(x, axis=-1, eps=1e-5):
    m = x.mean(dim=axis, keepdim=True)
    v = x.var(dim=axis, keepdim=True, unbiased=False)
    return (x - m) / torch.sqrt(v + eps)
def _scatter_add(tgt, idx, src, axis=0):
    return tgt.clone().index_add_(axis, idx, src)
def _segment_sum(src, seg, n):
    out = torch.zeros((n,) + src.shape[1:], dtype=src.dtype)
    return out.index_add_(0, seg, src)
def _pad(x, pw):
    flat = tuple(v for pair in reversed(pw) for v in pair)
    return torch.nn.functional.pad(x, flat)

kernel_name = 'gated_mix__grouped_kv_mqa__mha_block__prompt'

def setup_inputs():

    _w = (lambda a: (lambda t: t.to(torch.bfloat16) if t.is_floating_point() else t)(torch.as_tensor(a)))
    def _A_setup():
        B, S, D, H = 1, 2048, 512, 32
        Dh = D // H
        return {
            "x": _w((lambda *s: torch.randn(*s, dtype=torch.bfloat16) * 0.05)(B, S, D)),
            "wq": _w((lambda *s: torch.randn(*s, dtype=torch.bfloat16) * 1.6)(D, H * Dh)),
            "wk": _w((lambda *s: torch.randn(*s, dtype=torch.bfloat16) * 1.6)(D, Dh)),
            "wv": _w((lambda *s: torch.randn(*s, dtype=torch.bfloat16) * 0.8)(D, Dh)),
            "wo": _w((lambda *s: torch.randn(*s, dtype=torch.bfloat16) * 0.8)(H * Dh, D)),
        }
    _A_in = _A_setup()

    def _B_setup():
        B, S, D, H = 1, 2048, 512, 32
        return {
            "x": _w((lambda *s: torch.randn(*s, dtype=torch.bfloat16) * 0.05)(B, S, D)),
            "wqkv": _w((lambda *s: torch.randn(*s, dtype=torch.bfloat16) * 3.2)(D, 3 * D)),
            "wo": _w((lambda *s: torch.randn(*s, dtype=torch.bfloat16) * 0.05)(D, D)),
        }
    _B_in = _B_setup()

    return {
        "A_x": _A_in["x"],
        "A_wq": _A_in["wq"],
        "A_wk": _A_in["wk"],
        "A_wv": _A_in["wv"],
        "A_wo": _A_in["wo"],
        "B_x": _B_in["x"],
        "B_wqkv": _B_in["wqkv"],
        "B_wo": _B_in["wo"],
        "gate_w": _w((lambda *s: torch.randn(*s, dtype=torch.bfloat16) * 0.1)(512) * 16.0),
    }

def reference(A_x, A_wq, A_wk, A_wv, A_wo, B_x, B_wqkv, B_wo, gate_w):
    def _A_ref(x, wq, wk, wv, wo):
        B, S, D = x.shape
        H = 32
        Dh = D // H
        q = (lambda x, *d: x.permute(*d))((x @ wq).reshape(B, S, H, Dh), 0, 2, 1, 3)
        k = x @ wk
        v = x @ wv
        scores = torch.einsum("bhqd,bkd->bhqk", q, k) * (Dh ** -0.5)
        p = _softmax(scores, axis=-1)
        o = torch.einsum("bhqk,bkd->bhqd", p, v)
        return (lambda x, *d: x.permute(*d))(o, 0, 2, 1, 3).reshape(B, S, H * Dh) @ wo

    def _B_ref(x, wqkv, wo):
        B, S, D = x.shape
        H = 32
        Dh = D // H
        qkv = (lambda x, *d: x.permute(*d))((x @ wqkv).reshape(B, S, 3, H, Dh), 2, 0, 3, 1, 4)
        q, k, v = qkv[0], qkv[1], qkv[2]
        scores = torch.einsum("bhqd,bhkd->bhqk", q, k) * (Dh ** -0.5)
        p = _softmax(scores, axis=-1)
        o = torch.einsum("bhqk,bhkd->bhqd", p, v)
        return (lambda x, *d: x.permute(*d))(o, 0, 2, 1, 3).reshape(B, S, D) @ wo

    x = A_x
    g = _sigmoid((x * gate_w).sum(-1, keepdims=True))
    a = _A_ref(x, A_wq, A_wk, A_wv, A_wo)
    b = _B_ref(x, B_wqkv, B_wo)
    return g * a + (1 - g) * b
```

```cpp
#include <hip/hip_runtime.h>
#include <hip/hip_bf16.h>
#include <stdint.h>

typedef __attribute__((ext_vector_type(8))) __bf16 bf16x8;
typedef __attribute__((ext_vector_type(16))) __bf16 bf16x16;
typedef __attribute__((ext_vector_type(4))) short s16x4;
typedef __attribute__((ext_vector_type(8))) short s16x8;
typedef __attribute__((ext_vector_type(4))) float f32x4;
typedef __attribute__((ext_vector_type(8))) float f32x8;
typedef __attribute__((ext_vector_type(16))) float f32x16;
typedef __attribute__((ext_vector_type(4))) unsigned u32x4;
typedef __attribute__((ext_vector_type(2))) unsigned u32x2;
typedef unsigned short u16;
typedef __attribute__((address_space(3))) const char* lds_cptr;
typedef __attribute__((address_space(3))) char* lds_ptr;

constexpr int SEQ = 2048, DM = 512, NH = 32, DH = 16;
constexpr float QSCALE = 0.25f * 1.4426950408889634f;

constexpr size_t KV_TILE = 2080;
constexpr size_t KV_HEAD = 64 * KV_TILE;
constexpr size_t WS_Q = 0;
constexpr size_t WS_O = WS_Q + (size_t)64 * SEQ * DH * 2;
constexpr size_t WS_WO = WS_O + (size_t)64 * SEQ * DH * 2;
constexpr size_t WS_G = WS_WO + (size_t)1024 * 512 * 2;
constexpr size_t WS_KV = WS_G + 2 * SEQ * 4;
constexpr size_t WS_END = WS_KV + 34 * KV_HEAD;

#define SBAR() __builtin_amdgcn_sched_barrier(0)
#define MFMA32(a, b, c) __builtin_amdgcn_mfma_f32_32x32x16_bf16(a, b, c, 0, 0, 0)
#define MFMA16(a, b, c) __builtin_amdgcn_mfma_f32_16x16x32_bf16(a, b, c, 0, 0, 0)

__device__ __forceinline__ unsigned cvtpk(float lo, float hi) {
    unsigned r; asm("v_cvt_pk_bf16_f32 %0, %1, %2" : "=v"(r) : "v"(lo), "v"(hi)); return r;
}
__device__ __forceinline__ float bf2f(u16 v) { return __uint_as_float((unsigned)v << 16); }
__device__ __forceinline__ float rbf(float v) { return __uint_as_float(cvtpk(v, 0.f) << 16); }

__device__ __forceinline__ void glds16(const void* g, unsigned lds_base) {
    unsigned sv;
    asm volatile("s_mov_b32 %0, m0\n\ts_mov_b32 m0, %2\n\ts_nop 0\n\tglobal_load_lds_dwordx4 %1, off\n\ts_mov_b32 m0, %0"
                 : "=&s"(sv) : "v"(g), "s"(lds_base) : "memory");
}
__device__ __forceinline__ s16x4 ldtr(lds_cptr p) {
    typedef short v4i16_t __attribute__((ext_vector_type(4)));
    return __builtin_bit_cast(s16x4, __builtin_amdgcn_ds_read_tr16_b64_v4i16((__attribute__((address_space(3))) v4i16_t*)p));
}
__device__ __forceinline__ bf16x8 ld128(lds_cptr p) { return *(const __attribute__((address_space(3))) bf16x8*)p; }
__device__ __forceinline__ bf16x8 cat8(s16x4 a, s16x4 b) {
    s16x8 v = {a[0], a[1], a[2], a[3], b[0], b[1], b[2], b[3]}; return __builtin_bit_cast(bf16x8, v);
}
#define WAITV_BAR(N) asm volatile("s_waitcnt vmcnt(" #N ") lgkmcnt(0)\n\ts_barrier" ::: "memory")

constexpr int P_STAGE = 32768, P_NST = 4, P_EXTRA = P_NST * P_STAGE, P_LDS = P_EXTRA + 16384;

__device__ __forceinline__ unsigned wswz(unsigned row) { return ((row & 3) << 2) | ((row >> 2) & 3); }

__global__ __launch_bounds__(256, 1) void k_proj(const u16* __restrict__ x, const u16* __restrict__ wq, const u16* __restrict__ wk,
                                                 const u16* __restrict__ wv, const u16* __restrict__ wqkv, const u16* __restrict__ woA,
                                                 const u16* __restrict__ woB, const u16* __restrict__ gw, char* __restrict__ ws) {
    extern __shared__ __attribute__((aligned(16))) char smem[];
    const int tid = threadIdx.x, lane = tid & 63, wid = __builtin_amdgcn_readfirstlane(tid >> 6);
    const int bid = blockIdx.x, xcd = bid & 7, idx = bid >> 3;
    const int bi = (xcd >> 1) * 4 + (idx & 3), bj = (xcd & 1) * 8 + (idx >> 2);
    const unsigned lds0 = (unsigned)(uintptr_t)smem;
    const u16* W; int ldw, col0;
    if (bj < 4) { W = wq; ldw = 512; col0 = bj * 128; } else { W = wqkv; ldw = 1536; col0 = (bj - 4) * 128; }
    const u16* xs[4]; const u16* wsrc[4];
#pragma unroll
    for (int i = 0; i < 4; ++i) {
        const int p = wid + 4 * i;
        const int row = 8 * p + (lane >> 3), ch = (lane & 7) ^ ((row >> 1) & 7);
        xs[i] = x + (size_t)(bi * 128 + row) * DM + ch * 8;
        const int kr = 4 * p + (lane >> 4), wch = (lane & 15) ^ wswz(kr);
        wsrc[i] = W + (size_t)kr * ldw + col0 + wch * 8;
    }
    auto stage = [&](int kc) {
        const unsigned base = lds0 + (kc & (P_NST - 1)) * P_STAGE;
#pragma unroll
        for (int i = 0; i < 4; ++i) glds16(xs[i] + kc * 64, base + (wid + 4 * i) * 1024);
#pragma unroll
        for (int i = 0; i < 4; ++i) glds16(wsrc[i] + (size_t)kc * 64 * ldw, base + 16384 + (wid + 4 * i) * 1024);
    };
    {
        const u16* we = (bj & 1) ? wv : wk;
#pragma unroll
        for (int i = 0; i < 4; ++i) glds16(we + (wid + 4 * i) * 512 + lane * 8, lds0 + P_EXTRA + (wid + 4 * i) * 1024);
    }
    stage(0); stage(1); stage(2);
    {
        const int row = bi * 128 + bj * 8 + (tid >> 5), c0 = (tid & 31) * 16;
        const u32x4 xa = *(const u32x4*)(x + (size_t)row * DM + c0), xb = *(const u32x4*)(x + (size_t)row * DM + c0 + 8);
        const u32x4 ga = *(const u32x4*)(gw + c0), gb = *(const u32x4*)(gw + c0 + 8);
        float z = 0.f;
#pragma unroll
        for (int i = 0; i < 4; ++i) {
            z += rbf(__uint_as_float(xa[i] << 16) * __uint_as_float(ga[i] << 16));
            z += rbf(__uint_as_float(xa[i] & 0xffff0000u) * __uint_as_float(ga[i] & 0xffff0000u));
            z += rbf(__uint_as_float(xb[i] << 16) * __uint_as_float(gb[i] << 16));
            z += rbf(__uint_as_float(xb[i] & 0xffff0000u) * __uint_as_float(gb[i] & 0xffff0000u));
        }
#pragma unroll
        for (int o = 16; o >= 1; o >>= 1) z += __shfl_xor(z, o);
        z = rbf(z);
        const float g = rbf(1.f / (1.f + __expf(-z)));
        if ((tid & 31) == 0) { float* G = (float*)(ws + WS_G); G[row] = g; G[SEQ + row] = rbf(1.f - g); }
    }
    {
        const int ks = bid >> 2, nt = (bid & 3) * 4 + wid;
        const u16* wo = (ks < 32) ? woA + (size_t)ks * 16 * DM : woB + (size_t)(ks - 32) * 16 * DM;
        const u16* src = wo + (size_t)(8 * (lane >> 5)) * DM + nt * 32 + (lane & 31);
        u16 v[8];
#pragma unroll
        for (int j = 0; j < 8; ++j) v[j] = src[j * DM];
        u32x4 o = {v[0] | ((unsigned)v[1] << 16), v[2] | ((unsigned)v[3] << 16), v[4] | ((unsigned)v[5] << 16), v[6] | ((unsigned)v[7] << 16)};
        *(u32x4*)(ws + WS_WO + ((size_t)(ks * 16 + nt) * 64 + lane) * 16) = o;
    }
    const int wr = wid >> 1, wc = wid & 1, r = lane & 31, h = lane >> 5;
    const bool vtile = bj >= 12;
    f32x16 acc[2][2] = {};
    f32x4 eacc = {};
    unsigned xoff[2][4], woff[2][2][2];
#pragma unroll
    for (int m = 0; m < 2; ++m)
#pragma unroll
        for (int s = 0; s < 4; ++s) { const int row = wr * 64 + m * 32 + r; xoff[m][s] = row * 128 + 16 * ((2 * s + h) ^ ((row >> 1) & 7)); }
    {
        const int blk = (lane >> 4) & 1, q = (lane & 15) >> 2, p = lane & 3;
#pragma unroll
        for (int c = 0; c < 2; ++c)
#pragma unroll
            for (int t = 0; t < 2; ++t) {
                const int krow = 8 * h + 4 * t + q;
                woff[c][t][0] = 16384 + 256 * krow + 16 * ((4 * (wc * 2 + c) + 2 * blk + (p >> 1)) ^ wswz(krow)) + 8 * (p & 1);
                woff[c][t][1] = 0;
            }
    }
    const int erow = (bj >> 1) * 16 + (lane & 15), eg = lane >> 4;
    unsigned exoff[2];
#pragma unroll
    for (int s2 = 0; s2 < 2; ++s2) exoff[s2] = erow * 128 + 16 * ((4 * s2 + eg) ^ ((erow >> 1) & 7));
    const unsigned ewoff = P_EXTRA + (8 * eg + ((lane & 15) >> 2)) * 32 + 8 * (lane & 3);

    lds_cptr L = (lds_cptr)smem;
#pragma unroll 1
    for (int kc = 0; kc < 8; ++kc) {
        if (kc < 6) { WAITV_BAR(16); } else if (kc == 6) { WAITV_BAR(8); } else { WAITV_BAR(0); }
        if (kc + 3 < 8) stage(kc + 3);
        lds_cptr S = L + (kc & (P_NST - 1)) * P_STAGE;
#pragma unroll
        for (int s = 0; s < 4; ++s) {
            bf16x8 xf[2], wf[2];
#pragma unroll
            for (int m = 0; m < 2; ++m) xf[m] = ld128(S + xoff[m][s]);
#pragma unroll
            for (int c = 0; c < 2; ++c) wf[c] = cat8(ldtr(S + woff[c][0][0] + s * 4096), ldtr(S + woff[c][1][0] + s * 4096));
            if (vtile) {
#pragma unroll
                for (int m = 0; m < 2; ++m)
#pragma unroll
                    for (int c = 0; c < 2; ++c) acc[m][c] = MFMA32(xf[m], wf[c], acc[m][c]);
            } else {
#pragma unroll
                for (int m = 0; m < 2; ++m)
#pragma unroll
                    for (int c = 0; c < 2; ++c) acc[m][c] = MFMA32(wf[c], xf[m], acc[m][c]);
            }
        }
        if (wid == 0) {
#pragma unroll
            for (int s2 = 0; s2 < 2; ++s2) {
                const bf16x8 xe = ld128(S + exoff[s2]);
                lds_cptr wp = L + ewoff + (kc * 64 + s2 * 32) * 32;
                const bf16x8 we = cat8(ldtr(wp), ldtr(wp + 128));
                if (bj & 1) eacc = MFMA16(xe, we, eacc); else eacc = MFMA16(we, xe, eacc);
            }
        }
    }
    char* kvbase = ws + WS_KV;
    const u32x4 ones = {0x3f803f80u, 0x3f803f80u, 0x3f803f80u, 0x3f803f80u};
#pragma unroll
    for (int m = 0; m < 2; ++m)
#pragma unroll
        for (int c = 0; c < 2; ++c) {
            const f32x16 a = acc[m][c];
            const float sc = (bj < 8) ? QSCALE : 1.f;
            unsigned w[8];
#pragma unroll
            for (int i = 0; i < 8; ++i) w[i] = cvtpk(a[2 * i] * sc, a[2 * i + 1] * sc);
            u32x4 o[2];
#pragma unroll
            for (int u = 0; u < 2; ++u) {
                auto s0 = __builtin_amdgcn_permlane32_swap(w[4 * u + 0], w[4 * u + 2], false, false);
                auto s1 = __builtin_amdgcn_permlane32_swap(w[4 * u + 1], w[4 * u + 3], false, false);
                o[u] = (u32x4){s0[0], s1[0], s0[1], s1[1]};
            }
            const int srow0 = bi * 128 + wr * 64 + m * 32;
            const int f0 = (bj & 3) * 128 + wc * 64 + c * 32;
            const int hd0 = f0 >> 4;
            if (bj < 8) {
#pragma unroll
                for (int u = 0; u < 2; ++u) {
                    const int head = (bj >> 2) * 32 + hd0 + u;
                    *(u32x4*)(ws + WS_Q + ((size_t)(head * SEQ + srow0 + r) * 16 + 8 * h) * 2) = o[u];
                }
            } else if (bj < 12) {
#pragma unroll
                for (int u = 0; u < 2; ++u)
                    *(u32x4*)(kvbase + (size_t)(1 + hd0 + u) * KV_HEAD + (srow0 >> 5) * KV_TILE + h * 512 + r * 16) = o[u];
            } else {
                char* vb = kvbase + (size_t)(1 + hd0 + (r >> 4)) * KV_HEAD + (srow0 >> 5) * KV_TILE + 1024;
#pragma unroll
                for (int u = 0; u < 2; ++u) *(u32x4*)(vb + u * 528 + h * 256 + (r & 15) * 16) = o[u];
                if ((r & 15) == 0) *(u32x4*)(vb + h * 528 + 512) = ones;
            }
        }
    if (wid == 0) {
        const u32x2 e = {cvtpk(eacc[0], eacc[1]), cvtpk(eacc[2], eacc[3])};
        const int key0 = bi * 128 + (bj >> 1) * 16;
        char* tb = kvbase + (size_t)(key0 >> 5) * KV_TILE;
        const int u = (key0 >> 4) & 1;
        if (bj & 1) {
            *(u32x2*)(tb + 1024 + u * 528 + (eg >> 1) * 256 + (lane & 15) * 16 + (eg & 1) * 8) = e;
            if (lane == 0) *(u32x4*)(tb + 1024 + u * 528 + 512) = ones;
        } else {
            *(u32x2*)(tb + (eg >> 1) * 512 + (u * 16 + (lane & 15)) * 16 + (eg & 1) * 8) = e;
        }
    }
}

constexpr int A_PIECES = 136;
constexpr int A_QOFF = A_PIECES * 1024, A_LDS = A_QOFF + 16384;

#define SMF(acc, vf, p) acc = __builtin_amdgcn_smfmac_f32_32x32x32_bf16(vf, __builtin_bit_cast(bf16x16, p), acc, sidx, 0, 0)
#define EXPR(S, lo, hi) _Pragma("unroll") for (int e_ = lo; e_ < hi; ++e_) S[e_] = __builtin_amdgcn_exp2f(S[e_])
#define LO8(S) __builtin_shufflevector(S, S, 0, 1, 2, 3, 4, 5, 6, 7)
#define HI8(S) __builtin_shufflevector(S, S, 8, 9, 10, 11, 12, 13, 14, 15)

__device__ __forceinline__ float max16(const f32x16& s) {
    float a = fmaxf(fmaxf(s[0], s[1]), s[2]), b = fmaxf(fmaxf(s[3], s[4]), s[5]);
    a = fmaxf(fmaxf(a, s[6]), s[7]); b = fmaxf(fmaxf(b, s[8]), s[9]);
    a = fmaxf(fmaxf(a, s[10]), s[11]); b = fmaxf(fmaxf(b, s[12]), s[13]);
    a = fmaxf(fmaxf(a, s[14]), s[15]);
    return fmaxf(a, b);
}
__device__ __forceinline__ float xhalf_max(float m) {
    auto rr = __builtin_amdgcn_permlane32_swap(__float_as_uint(m), __float_as_uint(m), false, false);
    return fmaxf(__uint_as_float(rr[0]), __uint_as_float(rr[1]));
}

__global__ __launch_bounds__(512, 2) void k_attn(char* __restrict__ ws) {
    extern __shared__ __attribute__((aligned(16))) char smem[];
    const int tid = threadIdx.x, lane = tid & 63, wid = __builtin_amdgcn_readfirstlane(tid >> 6);
    const int bid = blockIdx.x, xcd = bid & 7, idx = bid >> 3;
    const int head = (idx >> 4) * 32 + xcd * 4 + ((idx >> 2) & 3), qb = idx & 3;
    const int kvh = head < 32 ? 0 : head - 31;
    const unsigned lds0 = (unsigned)(uintptr_t)smem;
    const int q0 = qb * 512 + wid * 64;
    {
        const char* qsrc = ws + WS_Q + (size_t)(head * SEQ + q0) * 32 + lane * 16;
        glds16(qsrc, lds0 + A_QOFF + wid * 2048);
        glds16(qsrc + 1024, lds0 + A_QOFF + wid * 2048 + 1024);
        const char* ksrc = ws + WS_KV + (size_t)kvh * KV_HEAD + wid * 1024 + lane * 16;
#pragma unroll
        for (int i = 0; i < 17; ++i) glds16(ksrc + i * 8192, lds0 + wid * 1024 + i * 8192);
    }
    const int r = lane & 31, h = lane >> 5;
    lds_cptr L = (lds_cptr)smem;
    lds_cptr kp = L + lane * 16;
    lds_cptr vp = L + 1024 + (r < 16 ? h * 256 + r * 16 : 512);
    int sidx = 0xDDDD; asm volatile("" : "+v"(sidx));
    WAITV_BAR(14);
    const bf16x8 qa = ld128(L + A_QOFF + wid * 2048 + r * 32 + h * 16);
    const bf16x8 qbf = ld128(L + A_QOFF + wid * 2048 + 1024 + r * 32 + h * 16);
    f32x16 OA, OB, CA, CB, SA, SB;
    float ma, mb;
    {
        const f32x16 z = {};
        const bf16x8 k0 = ld128(kp), k1 = ld128(kp + KV_TILE);
        ma = fmaxf(max16(MFMA32(k0, qa, z)), max16(MFMA32(k1, qa, z)));
        mb = fmaxf(max16(MFMA32(k0, qbf, z)), max16(MFMA32(k1, qbf, z)));
        ma = xhalf_max(ma); mb = xhalf_max(mb);
    }
    for (int attempt = 0; attempt < 2; ++attempt) {
#pragma unroll
        for (int i = 0; i < 16; ++i) { CA[i] = -ma; CB[i] = -mb; OA[i] = 0.f; OB[i] = 0.f; SB[i] = 0.f; }
        asm volatile("" : "+v"(CA), "+v"(CB));
        bf16x8 kf = ld128(kp), v0 = ld128(vp), v1 = ld128(vp + 528);
        SA = MFMA32(kf, qa, CA);
#pragma unroll 1
        for (int c = 0; c < 8; ++c) {
            if (attempt == 0) {
                switch (c) {
                    case 0: WAITV_BAR(14); break;
                    case 1: WAITV_BAR(12); break;
                    case 2: WAITV_BAR(10); break;
                    case 3: WAITV_BAR(8); break;
                    case 4: WAITV_BAR(6); break;
                    case 5: WAITV_BAR(4); break;
                    case 6: WAITV_BAR(2); break;
                    default: WAITV_BAR(0); break;
                }
            }
            lds_cptr kc = kp + c * 8 * KV_TILE;
            lds_cptr vc = vp + c * 8 * KV_TILE;
#pragma unroll
            for (int t = 0; t < 8; ++t) {
                SBAR();
                EXPR(SA, 0, 2);
                SBAR();
                SMF(OB, v1, HI8(SB));
                v1 = ld128(vc + t * KV_TILE + 528);
                SBAR();
                EXPR(SA, 2, 6);
                SBAR();
                SB = MFMA32(kf, qbf, CB);
                kf = ld128(kc + (t + 1) * KV_TILE);
                SBAR();
                EXPR(SA, 6, 10);
                SBAR();
                SMF(OA, v0, LO8(SA));
                SBAR();
                EXPR(SA, 10, 16);
                EXPR(SB, 0, 2);
                SBAR();
                SMF(OA, v1, HI8(SA));
                SBAR();
                EXPR(SB, 2, 6);
                SBAR();
                SA = MFMA32(kf, qa, CA);
                SBAR();
                EXPR(SB, 6, 10);
                SBAR();
                SMF(OB, v0, LO8(SB));
                v0 = ld128(vc + (t + 1) * KV_TILE);
                SBAR();
                EXPR(SB, 10, 16);
            }
        }
        SMF(OB, v1, HI8(SB));
        const float la = OA[8], lb = OB[8];
        float oa = 0.f, ob = 0.f;
#pragma unroll
        for (int i = 0; i < 8; ++i) { oa = fmaxf(oa, fabsf(OA[i])); ob = fmaxf(ob, fabsf(OB[i])); }
        const bool ok = (la <= 0x1p100f) && (lb <= 0x1p100f) && (oa <= 0x1p126f) && (ob <= 0x1p126f);
        if (attempt == 1 || __all(ok)) break;
        __builtin_amdgcn_s_waitcnt(0);
        const f32x16 z = {};
        float ta = -INFINITY, tb = -INFINITY;
#pragma unroll 1
        for (int t = 0; t < 64; ++t) {
            const bf16x8 k0 = ld128(kp + t * KV_TILE);
            ta = fmaxf(ta, max16(MFMA32(k0, qa, z)));
            tb = fmaxf(tb, max16(MFMA32(k0, qbf, z)));
        }
        ma = xhalf_max(ta); mb = xhalf_max(tb);
    }
    const float* G = (const float*)(ws + WS_G) + (head < 32 ? 0 : SEQ);
    {
        const float sa = G[q0 + r] / OA[8], sb = G[q0 + 32 + r] / OB[8];
        unsigned w[4];
#pragma unroll
        for (int i = 0; i < 4; ++i) w[i] = cvtpk(OA[2 * i] * sa, OA[2 * i + 1] * sa);
        auto s0 = __builtin_amdgcn_permlane32_swap(w[0], w[2], false, false);
        auto s1 = __builtin_amdgcn_permlane32_swap(w[1], w[3], false, false);
        *(u32x4*)(ws + WS_O + ((size_t)(head * SEQ + q0 + r) * 16 + 8 * h) * 2) = (u32x4){s0[0], s1[0], s0[1], s1[1]};
#pragma unroll
        for (int i = 0; i < 4; ++i) w[i] = cvtpk(OB[2 * i] * sb, OB[2 * i + 1] * sb);
        auto t0 = __builtin_amdgcn_permlane32_swap(w[0], w[2], false, false);
        auto t1 = __builtin_amdgcn_permlane32_swap(w[1], w[3], false, false);
        *(u32x4*)(ws + WS_O + ((size_t)(head * SEQ + q0 + 32 + r) * 16 + 8 * h) * 2) = (u32x4){t0[0], t1[0], t0[1], t1[1]};
    }
}

__global__ __launch_bounds__(256, 1) void k_out(const char* __restrict__ ws, u16* __restrict__ out) {
    __shared__ __attribute__((aligned(16))) float red[4][4][64 * 16 + 0];
    const int tid = threadIdx.x, lane = tid & 63, wid = __builtin_amdgcn_readfirstlane(tid >> 6);
    const int bid = blockIdx.x, xcd = bid & 7, idx = bid >> 3;
    const int bm = xcd * 4 + (idx & 3), bn = idx >> 2;
    const int r = lane & 31, h = lane >> 5;
    const char* Op = ws + WS_O + ((size_t)(wid * 16) * SEQ + bm * 64 + r) * 32 + h * 16;
    const char* Wp = ws + WS_WO + ((size_t)(wid * 16) * 16 + bn * 2) * 1024 + lane * 16;
    f32x16 acc[2][2] = {};
#pragma unroll
    for (int half = 0; half < 2; ++half) {
        bf16x8 a[8][2], b[8][2];
#pragma unroll
        for (int k = 0; k < 8; ++k) {
            const int ks = half * 8 + k;
            a[k][0] = *(const bf16x8*)(Op + (size_t)ks * SEQ * 32);
            a[k][1] = *(const bf16x8*)(Op + (size_t)ks * SEQ * 32 + 1024);
            b[k][0] = *(const bf16x8*)(Wp + (size_t)ks * 16384);
            b[k][1] = *(const bf16x8*)(Wp + (size_t)ks * 16384 + 1024);
        }
#pragma unroll
        for (int k = 0; k < 8; ++k)
#pragma unroll
            for (int m = 0; m < 2; ++m)
#pragma unroll
                for (int n = 0; n < 2; ++n) acc[m][n] = MFMA32(b[k][n], a[k][m], acc[m][n]);
    }
#pragma unroll
    for (int m = 0; m < 2; ++m)
#pragma unroll
        for (int n = 0; n < 2; ++n)
#pragma unroll
            for (int g = 0; g < 4; ++g)
                *(f32x4*)&red[wid][m * 2 + n][(g * 64 + lane) * 4] = (f32x4){acc[m][n][4 * g], acc[m][n][4 * g + 1], acc[m][n][4 * g + 2], acc[m][n][4 * g + 3]};
    __syncthreads();
    {
        f32x4 s[4];
#pragma unroll
        for (int g = 0; g < 4; ++g) {
            s[g] = *(const f32x4*)&red[0][wid][(g * 64 + lane) * 4];
#pragma unroll
            for (int w = 1; w < 4; ++w) s[g] += *(const f32x4*)&red[w][wid][(g * 64 + lane) * 4];
        }
        unsigned w[8];
#pragma unroll
        for (int g = 0; g < 4; ++g) { w[2 * g] = cvtpk(s[g][0], s[g][1]); w[2 * g + 1] = cvtpk(s[g][2], s[g][3]); }
        u16* orow = out + (size_t)(bm * 64 + (wid >> 1) * 32 + r) * DM + bn * 64 + (wid & 1) * 32;
#pragma unroll
        for (int u = 0; u < 2; ++u) {
            auto s0 = __builtin_amdgcn_permlane32_swap(w[4 * u + 0], w[4 * u + 2], false, false);
            auto s1 = __builtin_amdgcn_permlane32_swap(w[4 * u + 1], w[4 * u + 3], false, false);
            *(u32x4*)(orow + u * 16 + h * 8) = (u32x4){s0[0], s1[0], s0[1], s1[1]};
        }
    }
}

extern "C" void kernel_launch(void* const* d_in, const int* in_sizes, int n_in,
                              void* d_out, int out_size, void* d_ws, size_t ws_size,
                              hipStream_t stream) {
    const u16* x = (const u16*)d_in[0];
    const u16* wq = (const u16*)d_in[1];
    const u16* wk = (const u16*)d_in[2];
    const u16* wv = (const u16*)d_in[3];
    const u16* woA = (const u16*)d_in[4];
    const u16* wqkv = (const u16*)d_in[6];
    const u16* woB = (const u16*)d_in[7];
    const u16* gw = (const u16*)d_in[8];
    char* ws = (char*)d_ws;
    (void)hipFuncSetAttribute((const void*)k_proj, hipFuncAttributeMaxDynamicSharedMemorySize, P_LDS);
    (void)hipFuncSetAttribute((const void*)k_attn, hipFuncAttributeMaxDynamicSharedMemorySize, A_LDS);
    k_proj<<<256, 256, P_LDS, stream>>>(x, wq, wk, wv, wqkv, woA, woB, gw, ws);
    k_attn<<<256, 512, A_LDS, stream>>>(ws);
    k_out<<<256, 256, 0, stream>>>(ws, (u16*)d_out);
}
```

```cpp
#include <hip/hip_runtime.h>
#include <hip/hip_bf16.h>
#include <stdint.h>

typedef __attribute__((ext_vector_type(8))) __bf16 bf16x8;
typedef __attribute__((ext_vector_type(16))) __bf16 bf16x16;
typedef __attribute__((ext_vector_type(4))) short s16x4;
typedef __attribute__((ext_vector_type(8))) short s16x8;
typedef __attribute__((ext_vector_type(4))) float f32x4;
typedef __attribute__((ext_vector_type(8))) float f32x8;
typedef __attribute__((ext_vector_type(16))) float f32x16;
typedef __attribute__((ext_vector_type(4))) unsigned u32x4;
typedef __attribute__((ext_vector_type(2))) unsigned u32x2;
typedef unsigned short u16;
typedef __attribute__((address_space(3))) const char* lds_cptr;
typedef __attribute__((address_space(3))) char* lds_ptr;

constexpr int SEQ = 2048, DM = 512, NH = 32, DH = 16;
constexpr float QSCALE = 0.25f * 1.4426950408889634f;

constexpr int VBLK = 544;
constexpr size_t KV_TILE = 1024 + 2 * VBLK;
constexpr size_t KV_HEAD = 64 * KV_TILE;
constexpr size_t WS_Q = 0;
constexpr size_t WS_O = WS_Q + (size_t)64 * SEQ * DH * 2;
constexpr size_t WS_WO = WS_O + (size_t)64 * SEQ * DH * 2;
constexpr size_t WS_G = WS_WO + (size_t)1024 * 512 * 2;
constexpr size_t WS_CNT = WS_G + 2 * SEQ * 4;
constexpr size_t WS_KV = WS_CNT + 64 * 256;
constexpr size_t WS_END = WS_KV + 34 * KV_HEAD;

#define SBAR() __builtin_amdgcn_sched_barrier(0)
#define MFMA32(a, b, c) __builtin_amdgcn_mfma_f32_32x32x16_bf16(a, b, c, 0, 0, 0)
#define MFMA16(a, b, c) __builtin_amdgcn_mfma_f32_16x16x32_bf16(a, b, c, 0, 0, 0)

typedef __attribute__((ext_vector_type(2))) __bf16 bf16x2;
typedef __attribute__((ext_vector_type(2))) float f32x2;
__device__ __forceinline__ unsigned cvtpk(float lo, float hi) {
    return __builtin_bit_cast(unsigned, __builtin_convertvector((f32x2){lo, hi}, bf16x2));
}
#define STAMP(i)
#define STAMP_DECL
#define STAMP_OUT(k)
__device__ __forceinline__ float bf2f(u16 v) { return __uint_as_float((unsigned)v << 16); }
__device__ __forceinline__ float rbf(float v) { return __uint_as_float(cvtpk(v, 0.f) << 16); }

__device__ __forceinline__ void glds16(const void* g, unsigned lds_base) {
    unsigned sv;
    asm volatile("s_mov_b32 %0, m0\n\ts_mov_b32 m0, %2\n\ts_nop 0\n\tglobal_load_lds_dwordx4 %1, off\n\ts_mov_b32 m0, %0"
                 : "=&s"(sv) : "v"(g), "s"(lds_base) : "memory");
}
__device__ __forceinline__ s16x4 ldtr(lds_cptr p) {
    typedef short v4i16_t __attribute__((ext_vector_type(4)));
    return __builtin_bit_cast(s16x4, __builtin_amdgcn_ds_read_tr16_b64_v4i16((__attribute__((address_space(3))) v4i16_t*)p));
}
__device__ __forceinline__ bf16x8 ld128(lds_cptr p) { return *(const __attribute__((address_space(3))) bf16x8*)p; }
__device__ __forceinline__ bf16x8 cat8(s16x4 a, s16x4 b) {
    s16x8 v = {a[0], a[1], a[2], a[3], b[0], b[1], b[2], b[3]}; return __builtin_bit_cast(bf16x8, v);
}
#define ST16_PLAIN(p, v) (*(u32x4*)(p) = (v))
#define ST16_NT(p, v) __builtin_nontemporal_store((u32x4)(v), (u32x4*)(p))
#define ST16_SC1(p, v) do { u32x4 v_ = (v); asm volatile("global_store_dwordx4 %0, %1, off sc1\n\ts_nop 1" :: "v"(p), "v"(v_) : "memory"); } while (0)
#define ST16A ST16_NT
#define ST16B ST16_PLAIN
#define WAITV_BAR(N) asm volatile("s_waitcnt vmcnt(" #N ") lgkmcnt(0)\n\ts_barrier" ::: "memory")

constexpr int P_STAGE = 32768, P_NST = 4, P_EXTRA = P_NST * P_STAGE, P_GW = P_EXTRA + 16384, P_WO = P_GW + 1024, P_LDS = P_WO + 4096;

__device__ __forceinline__ unsigned wswz(unsigned row) { return ((row & 3) << 2) | ((row >> 2) & 3); }
#define LBAR() asm volatile("s_waitcnt lgkmcnt(0)\n\ts_barrier" ::: "memory")

template <bool VT, bool EV>
__device__ __forceinline__ void proj_loop(f32x16 (&acc)[2][2], f32x4& eacc, f32x4& zacc, lds_cptr L, int rot,
                                          const unsigned (&xoff)[2][4], const unsigned (&woff)[2][2], const unsigned (&exoff)[2],
                                          unsigned ewoff, unsigned gwoff) {
    bf16x8 xf[2][2], wf[2][2], xe, we, ge;
#define PMM(m, c, buf) acc[m][c] = VT ? MFMA32(xf[buf][m], wf[buf][c], acc[m][c]) : MFMA32(wf[buf][c], xf[buf][m], acc[m][c])
#define PRD_X(j, m) xf[(j) & 1][m] = ld128(L + (((j) >> 2) & 3) * P_STAGE + xoff[m][(j) & 3])
#define PRD_W(j, c) wf[(j) & 1][c] = cat8(ldtr(L + (((j) >> 2) & 3) * P_STAGE + woff[c][0] + ((j) & 3) * 4096), ldtr(L + (((j) >> 2) & 3) * P_STAGE + woff[c][1] + ((j) & 3) * 4096))
    LBAR();
    PRD_X(0, 0); PRD_W(0, 0); PRD_X(0, 1); PRD_W(0, 1);
#pragma unroll
    for (int j = 0; j < 32; ++j) {
        const int jn = j + 1;
        SBAR();
        if ((jn & 3) == 0 && jn < 32) LBAR();
        PMM(0, 0, j & 1);
        SBAR();
        if (jn < 32) { PRD_X(jn, 0); PRD_W(jn, 0); }
        SBAR();
        PMM(0, 1, j & 1);
        SBAR();
        if (jn < 32) { PRD_X(jn, 1); PRD_W(jn, 1); }
        SBAR();
        PMM(1, 0, j & 1);
        SBAR();
        if ((j & 1) == 0) {
            const int kg = ((((j >> 2) + rot) & 7) * 2 + ((j >> 1) & 1));
            xe = ld128(L + ((j >> 2) & 3) * P_STAGE + exoff[(j >> 1) & 1]);
            lds_cptr wp = L + ewoff + kg * 1024;
            we = cat8(ldtr(wp), ldtr(wp + 128));
            if (!EV) ge = ld128(L + gwoff + kg * 64);
        } else {
            eacc = EV ? MFMA16(xe, we, eacc) : MFMA16(we, xe, eacc);
            if (!EV) zacc = MFMA16(xe, ge, zacc);
        }
        SBAR();
        PMM(1, 1, j & 1);
    }
#undef PMM
#undef PRD_X
#undef PRD_W
}

__global__ __launch_bounds__(512, 2) void k_proj(const u16* __restrict__ x, const u16* __restrict__ wq, const u16* __restrict__ wk,
                                                 const u16* __restrict__ wv, const u16* __restrict__ wqkv, const u16* __restrict__ woA,
                                                 const u16* __restrict__ woB, const u16* __restrict__ gw, char* __restrict__ ws) {
    extern __shared__ __attribute__((aligned(16))) char smem[];
    const int tid = threadIdx.x, lane = tid & 63, wid = __builtin_amdgcn_readfirstlane(tid >> 6);
    const int bid = blockIdx.x, grp = bid & 7, idx = bid >> 3;
    const int cj = idx >> 2;
    const int bi = (grp >> 1) * 4 + (idx & 3);
    const int bj = (cj >> 1) * 4 + (grp & 1) * 2 + (cj & 1);
    const int rot = 0;
    const unsigned lds0 = (unsigned)(uintptr_t)smem;
    STAMP_DECL; STAMP(0);
    if (wid >= 4) {
        const int lw = wid - 4;
        const u16* W; int ldw, col0;
        if (bj < 4) { W = wq; ldw = 512; col0 = bj * 128; } else { W = wqkv; ldw = 1536; col0 = (bj - 4) * 128; }
        const u16* xs[4]; const u16* wsrc[4];
#pragma unroll
        for (int i = 0; i < 4; ++i) {
            const int p = lw + 4 * i;
            const int row = 8 * p + (lane >> 3), ch = (lane & 7) ^ ((row >> 1) & 7);
            xs[i] = x + (size_t)(bi * 128 + row) * DM + ch * 8;
            const int kr = 4 * p + (lane >> 4), wch = (lane & 15) ^ wswz(kr);
            wsrc[i] = W + (size_t)kr * ldw + col0 + wch * 8;
        }
#define PSTAGE(s) do { const int kc_ = ((s) + rot) & 7; const unsigned base_ = lds0 + ((s) & (P_NST - 1)) * P_STAGE + lw * 1024; \
        _Pragma("unroll") for (int i = 0; i < 4; ++i) { glds16(xs[i] + kc_ * 64, base_ + i * 4096); glds16(wsrc[i] + kc_ * 64 * ldw, base_ + 16384 + i * 4096); } } while (0)
        PSTAGE(0);
        {
            const u16* we = (bj & 1) ? wv : wk;
#pragma unroll
            for (int i = 0; i < 4; ++i) glds16(we + (lw + 4 * i) * 512 + lane * 8, lds0 + P_EXTRA + (lw + 4 * i) * 1024);
            glds16(gw + lane * 8, lds0 + P_GW);
        }
        PSTAGE(1);
        STAMP(4);
        WAITV_BAR(8);  PSTAGE(2); PSTAGE(3);
        WAITV_BAR(16); PSTAGE(4);
        WAITV_BAR(16); PSTAGE(5);
        WAITV_BAR(16); PSTAGE(6);
        WAITV_BAR(16); PSTAGE(7);
        {
            const int ks = bid >> 2, kr = 4 * lw + (lane >> 4);
            const u16* wo = (ks < 32) ? woA + (size_t)ks * 16 * DM : woB + (size_t)(ks - 32) * 16 * DM;
            glds16(wo + (size_t)kr * DM + (bid & 3) * 128 + ((lane & 15) ^ wswz(kr)) * 8, lds0 + P_WO + lw * 1024);
        }
        WAITV_BAR(17);
        WAITV_BAR(9);
        WAITV_BAR(0);
        {
            const int h = lane >> 5, blk = (lane >> 4) & 1, q = (lane & 15) >> 2, p = lane & 3;
            const int kr0 = 8 * h + q, kr1 = kr0 + 4, ch = 4 * lw + 2 * blk + (p >> 1);
            lds_cptr wb = (lds_cptr)smem + P_WO + 8 * (p & 1);
            *(bf16x8*)(ws + WS_WO + ((size_t)((bid >> 2) * 16 + (bid & 3) * 4 + lw) * 64 + lane) * 16) =
                cat8(ldtr(wb + 256 * kr0 + 16 * (ch ^ wswz(kr0))), ldtr(wb + 256 * kr1 + 16 * (ch ^ wswz(kr1))));
            if (bid == 0 && lw == 0) *(unsigned*)(ws + WS_CNT + lane * 256) = 0u;
        }
        STAMP(5); STAMP_OUT(0);
        return;
    }
    const int wr = wid >> 1, wc = wid & 1, r = lane & 31, h = lane >> 5;
    f32x16 acc[2][2] = {};
    f32x4 eacc = {}, zacc = {};
    unsigned xoff[2][4], woff[2][2];
#pragma unroll
    for (int m = 0; m < 2; ++m)
#pragma unroll
        for (int s = 0; s < 4; ++s) { const int row = wr * 64 + m * 32 + r; xoff[m][s] = row * 128 + 16 * ((2 * s + h) ^ ((row >> 1) & 7)); }
    {
        const int blk = (lane >> 4) & 1, q = (lane & 15) >> 2, p = lane & 3;
#pragma unroll
        for (int t = 0; t < 2; ++t) {
            const int krow = 8 * h + 4 * t + q;
#pragma unroll
            for (int c = 0; c < 2; ++c)
                woff[c][t] = 16384 + 256 * krow + 16 * ((4 * (wc * 2 + c) + 2 * blk + (p >> 1)) ^ wswz(krow)) + 8 * (p & 1);
        }
    }
    const int erow = (bj >> 1) * 16 + (lane & 15), eg = lane >> 4;
    unsigned exoff[2];
#pragma unroll
    for (int s2 = 0; s2 < 2; ++s2) exoff[s2] = erow * 128 + 16 * ((4 * s2 + eg) ^ ((erow >> 1) & 7));
    const unsigned ewoff = P_EXTRA + (8 * eg + ((lane & 15) >> 2)) * 32 + 8 * (lane & 3);
    const unsigned gwoff = P_GW + eg * 16;
    lds_cptr L = (lds_cptr)smem;
    STAMP(1);
    if (bj >= 12) {
        if (bj & 1) proj_loop<true, true>(acc, eacc, zacc, L, rot, xoff, woff, exoff, ewoff, gwoff);
        else proj_loop<true, false>(acc, eacc, zacc, L, rot, xoff, woff, exoff, ewoff, gwoff);
    } else {
        if (bj & 1) proj_loop<false, true>(acc, eacc, zacc, L, rot, xoff, woff, exoff, ewoff, gwoff);
        else proj_loop<false, false>(acc, eacc, zacc, L, rot, xoff, woff, exoff, ewoff, gwoff);
    }
    STAMP(2);
    char* kvbase = ws + WS_KV;
    const u32x4 ones = {0x3f803f80u, 0x3f803f80u, 0x3f803f80u, 0x3f803f80u};
    const float sc = (bj < 8) ? QSCALE : 1.f;
#pragma unroll
    for (int m = 0; m < 2; ++m)
#pragma unroll
        for (int c = 0; c < 2; ++c) {
            const f32x16 a = acc[m][c];
            unsigned w[8];
#pragma unroll
            for (int i = 0; i < 8; ++i) w[i] = cvtpk(a[2 * i] * sc, a[2 * i + 1] * sc);
            u32x4 o[2];
#pragma unroll
            for (int u = 0; u < 2; ++u) {
                auto s0 = __builtin_amdgcn_permlane32_swap(w[4 * u + 0], w[4 * u + 2], false, false);
                auto s1 = __builtin_amdgcn_permlane32_swap(w[4 * u + 1], w[4 * u + 3], false, false);
                o[u] = (u32x4){s0[0], s1[0], s0[1], s1[1]};
            }
            const int srow0 = bi * 128 + wr * 64 + m * 32;
            const int f0 = (bj & 3) * 128 + wc * 64 + c * 32;
            const int hd0 = f0 >> 4;
            if (bj < 8) {
#pragma unroll
                for (int u = 0; u < 2; ++u) {
                    const int head = (bj >> 2) * 32 + hd0 + u;
                    ST16A(ws + WS_Q + ((size_t)(head * SEQ + srow0 + r) * 16 + 8 * h) * 2, o[u]);
                }
            } else if (bj < 12) {
#pragma unroll
                for (int u = 0; u < 2; ++u)
                    ST16A(kvbase + (size_t)(1 + hd0 + u) * KV_HEAD + (srow0 >> 5) * KV_TILE + h * 512 + r * 16, o[u]);
            } else {
                char* vb = kvbase + (size_t)(1 + hd0 + (r >> 4)) * KV_HEAD + (srow0 >> 5) * KV_TILE + 1024;
#pragma unroll
                for (int u = 0; u < 2; ++u) ST16A(vb + u * VBLK + h * 256 + (r & 15) * 16, o[u]);
                if ((r & 15) < 2) *(u32x4*)(vb + h * VBLK + 512 + (r & 1) * 16) = (r & 1) ? (u32x4){0u, 0u, 0u, 0u} : ones;
            }
        }
    const int key0 = bi * 128 + (bj >> 1) * 16;
    if (wid == 0) {
        const u32x2 e = {cvtpk(eacc[0], eacc[1]), cvtpk(eacc[2], eacc[3])};
        char* tb = kvbase + (size_t)(key0 >> 5) * KV_TILE;
        const int u = (key0 >> 4) & 1;
        if (bj & 1) {
            *(u32x2*)(tb + 1024 + u * VBLK + (eg >> 1) * 256 + (lane & 15) * 16 + (eg & 1) * 8) = e;
            if (lane < 2) *(u32x4*)(tb + 1024 + u * VBLK + 512 + lane * 16) = lane ? (u32x4){0u, 0u, 0u, 0u} : ones;
        } else {
            *(u32x2*)(tb + (eg >> 1) * 512 + (u * 16 + (lane & 15)) * 16 + (eg & 1) * 8) = e;
        }
    }
    if (wid == 1 && !(bj & 1) && (lane & 15) == 0) {
        f32x4 g, og;
#pragma unroll
        for (int i = 0; i < 4; ++i) { g[i] = rbf(1.f / (1.f + __expf(-rbf(zacc[i])))); og[i] = rbf(1.f - g[i]); }
        float* G = (float*)(ws + WS_G);
        *(f32x4*)(G + key0 + 4 * eg) = g; *(f32x4*)(G + SEQ + key0 + 4 * eg) = og;
    }
    STAMP(3); STAMP_OUT(0);
}

constexpr int A_RED = (int)KV_HEAD;
constexpr int A_SYNC = A_RED + 24576, A_LDS = A_SYNC + 64;

#define PIN(x) asm volatile("" : "+v"(x))
#define SMF(acc, vf, p) do { acc = __builtin_amdgcn_smfmac_f32_32x32x32_bf16(vf, __builtin_bit_cast(bf16x16, p), acc, sidx, 0, 0); PIN(acc); } while (0)
#define EXPR(S, lo, hi) _Pragma("unroll") for (int e_ = lo; e_ < hi; ++e_) S[e_] = __builtin_amdgcn_exp2f(S[e_])
#define LO8(S) __builtin_shufflevector(S, S, 0, 1, 2, 3, 4, 5, 6, 7)
#define HI8(S) __builtin_shufflevector(S, S, 8, 9, 10, 11, 12, 13, 14, 15)

__device__ __forceinline__ float max16(const f32x16& s) {
    float a = fmaxf(fmaxf(s[0], s[1]), s[2]), b = fmaxf(fmaxf(s[3], s[4]), s[5]);
    a = fmaxf(fmaxf(a, s[6]), s[7]); b = fmaxf(fmaxf(b, s[8]), s[9]);
    a = fmaxf(fmaxf(a, s[10]), s[11]); b = fmaxf(fmaxf(b, s[12]), s[13]);
    a = fmaxf(fmaxf(a, s[14]), s[15]);
    return fmaxf(a, b);
}
__device__ __forceinline__ float xhalf_max(float m) {
    auto rr = __builtin_amdgcn_permlane32_swap(__float_as_uint(m), __float_as_uint(m), false, false);
    return fmaxf(__uint_as_float(rr[0]), __uint_as_float(rr[1]));
}

#define ATT_STEP(KN, V1C, V0N, G0, G1) do { \
    SBAR(); EXPR(SA, 0, 2);   SBAR(); SMF(OB, v1, HI8(SB)); v1 = ld128(V1C); \
    SBAR(); EXPR(SA, 2, 6);   SBAR(); SB = MFMA32(kf, qbf, CB); PIN(SB); kf = ld128(KN); \
    SBAR(); EXPR(SA, 6, 10);  SBAR(); SMF(OA, v0, LO8(SA)); G0; \
    SBAR(); EXPR(SA, 10, 16); EXPR(SB, 0, 2); SBAR(); SMF(OA, v1, HI8(SA)); \
    SBAR(); EXPR(SB, 2, 6);   SBAR(); SA = MFMA32(kf, qa, CA); PIN(SA); G1; \
    SBAR(); EXPR(SB, 6, 10);  SBAR(); SMF(OB, v0, LO8(SB)); v0 = ld128(V0N); \
    SBAR(); EXPR(SB, 10, 16); } while (0)

typedef __attribute__((address_space(1))) unsigned gu32;
typedef __attribute__((address_space(3))) unsigned lu32;
__device__ __forceinline__ unsigned lds_ld(lu32* p) { return __builtin_amdgcn_readfirstlane(__hip_atomic_load(p, __ATOMIC_RELAXED, __HIP_MEMORY_SCOPE_WORKGROUP)); }
__device__ __forceinline__ void lds_arrive_wait(lu32* w, unsigned target, int lane) {
    asm volatile("s_waitcnt lgkmcnt(0)" ::: "memory");
    if (lane == 0) __hip_atomic_fetch_add(w, 1u, __ATOMIC_RELAXED, __HIP_MEMORY_SCOPE_WORKGROUP);
    for (unsigned spins = 0; spins < (1u << 22); ++spins) { if (lds_ld(w) >= target) break; __builtin_amdgcn_s_sleep(1); }
    asm volatile("" ::: "memory");
}

__global__ __launch_bounds__(512, 2) void k_attn(char* __restrict__ ws, u16* __restrict__ out) {
    extern __shared__ __attribute__((aligned(16))) char smem[];
    const int tid = threadIdx.x, lane = tid & 63, wid = __builtin_amdgcn_readfirstlane(tid >> 6);
    const int bid = blockIdx.x, grp = bid & 7, idx = bid >> 3;
    const int head = (idx >> 4) * 32 + (grp & 1) * 16 + (idx & 15), qb = grp >> 1;
    const int kvh = head < 32 ? 0 : head - 31;
    const unsigned lds0 = (unsigned)(uintptr_t)smem;
    const int q0 = qb * 512 + wid * 64;
    STAMP_DECL; STAMP(0);
    const int r = lane & 31, h = lane >> 5;
    bf16x8 qa, qbf; float ga, gb;
    {
        const char* qsrc = ws + WS_Q + (size_t)(head * SEQ + q0 + r) * 32 + h * 16;
        const float* gsrc = (const float*)(ws + WS_G) + (head < 32 ? 0 : SEQ) + q0 + r;
        asm volatile("global_load_dwordx4 %0, %4, off\n\tglobal_load_dwordx4 %1, %4, off offset:1024\n\t"
                     "global_load_dword %2, %5, off\n\tglobal_load_dword %3, %5, off offset:128"
                     : "=&v"(qa), "=&v"(qbf), "=&v"(ga), "=&v"(gb) : "v"(qsrc), "v"(gsrc) : "memory");
    }
    const char* ksrc = ws + WS_KV + (size_t)kvh * KV_HEAD + lane * 16;
    const int krot = qb * 33 + wid;
    const unsigned kdst = lds0 + wid * 1024;
#define KVDMA(i) do { int p_ = krot + (i) * 8; p_ = p_ >= 132 ? p_ - 132 : p_; glds16(ksrc + p_ * 1024, kdst + (i) * 8192); } while (0)
    KVDMA(0); KVDMA(1); KVDMA(2);
    STAMP(14);
    const __amdgpu_buffer_rsrc_t rsO = __builtin_amdgcn_make_buffer_rsrc((void*)(ws + WS_O), 0, 64 * SEQ * DH * 2, 0x00020000);
    const unsigned ooff = (unsigned)(head * SEQ + q0 + r) * 32 + h * 16;
    const u32x4 poison = {~0u, ~0u, ~0u, ~0u};
    lds_cptr L = (lds_cptr)smem;
    lu32* sync = (lu32*)(smem + A_SYNC);
    if (tid < 16) sync[tid] = 0u;
    lds_cptr kp = L + lane * 16;
    lds_cptr vp = L + 1024 + (r < 16 ? h * 256 + r * 16 : ((r == 16 || r == 20) ? 512 : 528));
    int sidx = 0xDDDD; PIN(sidx);
    f32x16 OA, OB, CA, CB, SA, SB;
#pragma unroll
    for (int i = 0; i < 16; ++i) { OA[i] = 0.f; OB[i] = 0.f; SB[i] = 0.f; }
    asm volatile("" : "+v"(OA), "+v"(OB), "+v"(SB));
    asm volatile("s_waitcnt vmcnt(2) lgkmcnt(0)\n\ts_barrier" : "+v"(qa), "+v"(qbf), "+v"(ga), "+v"(gb) :: "memory");
    STAMP(1);
    float ma, mb;
    bf16x8 kf, v0, v1;
    {
        const f32x16 z = {};
        kf = ld128(kp);
        const bf16x8 k1 = ld128(kp + KV_TILE);
        v0 = ld128(vp); v1 = ld128(vp + VBLK);
        const f32x16 s0 = MFMA32(kf, qa, z), s1 = MFMA32(k1, qa, z), s2 = MFMA32(kf, qbf, z), s3 = MFMA32(k1, qbf, z);
        ma = xhalf_max(fmaxf(max16(s0), max16(s1)));
        mb = xhalf_max(fmaxf(max16(s2), max16(s3)));
    }
    for (int attempt = 0; attempt < 2; ++attempt) {
#pragma unroll
        for (int i = 0; i < 16; ++i) { CA[i] = -ma; CB[i] = -mb; }
        asm volatile("" : "+v"(CA), "+v"(CB));
        SA = MFMA32(kf, qa, CA);
        int c0 = 0;
        if (attempt == 0) {
            WAITV_BAR(0);
            STAMP(2);
            ATT_STEP(kp + 1 * KV_TILE, vp + 0 * KV_TILE + VBLK, vp + 1 * KV_TILE, KVDMA(3), KVDMA(4));
            ATT_STEP(kp + 2 * KV_TILE, vp + 1 * KV_TILE + VBLK, vp + 2 * KV_TILE, KVDMA(5), KVDMA(6));
            ATT_STEP(kp + 3 * KV_TILE, vp + 2 * KV_TILE + VBLK, vp + 3 * KV_TILE, KVDMA(7), KVDMA(8));
            ATT_STEP(kp + 4 * KV_TILE, vp + 3 * KV_TILE + VBLK, vp + 4 * KV_TILE, KVDMA(9), KVDMA(10));
            ATT_STEP(kp + 5 * KV_TILE, vp + 4 * KV_TILE + VBLK, vp + 5 * KV_TILE, KVDMA(11), KVDMA(12));
            ATT_STEP(kp + 6 * KV_TILE, vp + 5 * KV_TILE + VBLK, vp + 6 * KV_TILE, KVDMA(13), KVDMA(14));
            ATT_STEP(kp + 7 * KV_TILE, vp + 6 * KV_TILE + VBLK, vp + 7 * KV_TILE, KVDMA(15), if (wid < 4) KVDMA(16));
            ATT_STEP(kp + 8 * KV_TILE, vp + 7 * KV_TILE + VBLK, vp + 8 * KV_TILE,
                     if (wid >= 4) __builtin_amdgcn_raw_buffer_store_b128(poison, rsO, ooff, 0, 16),
                     if (wid >= 4) __builtin_amdgcn_raw_buffer_store_b128(poison, rsO, ooff + 1024, 0, 16));
            SBAR();
            asm volatile("s_waitcnt vmcnt(0)\n\ts_barrier" ::: "memory");
            STAMP(6);
            c0 = 1;
        }
#pragma unroll 1
        for (int c = c0; c < 8; ++c) {
            lds_cptr kc = kp + c * 8 * KV_TILE;
            lds_cptr vc = vp + c * 8 * KV_TILE;
#pragma unroll
            for (int t = 0; t < 8; ++t) ATT_STEP(kc + (t + 1) * KV_TILE, vc + t * KV_TILE + VBLK, vc + (t + 1) * KV_TILE, (void)0, (void)0);
        }
        SBAR();
        SMF(OB, v1, HI8(SB));
        if (attempt == 0) STAMP(3); else STAMP(5);
        const float la = OA[8], lb = OB[8];
        float oa = 0.f, ob = 0.f;
#pragma unroll
        for (int i = 0; i < 8; ++i) { oa += fabsf(OA[i]); ob += fabsf(OB[i]); }
        const bool ok = (la <= 0x1p100f) && (lb <= 0x1p100f) && (oa <= 0x1p126f) && (ob <= 0x1p126f);
        if (attempt == 1 || __all(ok)) break;
        const f32x16 z = {};
        float ta = -INFINITY, tb = -INFINITY;
#pragma unroll 1
        for (int t = 0; t < 64; ++t) {
            const bf16x8 k0 = ld128(kp + t * KV_TILE);
            ta = fmaxf(ta, max16(MFMA32(k0, qa, z)));
            tb = fmaxf(tb, max16(MFMA32(k0, qbf, z)));
        }
        ma = xhalf_max(ta); mb = xhalf_max(tb);
#pragma unroll
        for (int i = 0; i < 16; ++i) { OA[i] = 0.f; OB[i] = 0.f; SB[i] = 0.f; }
        kf = ld128(kp); v0 = ld128(vp); v1 = ld128(vp + VBLK);
    }
    {
        const float sa = ga * __builtin_amdgcn_rcpf(OA[8]), sb = gb * __builtin_amdgcn_rcpf(OB[8]);
        unsigned w[4];
#pragma unroll
        for (int i = 0; i < 4; ++i) w[i] = min(cvtpk(OA[2 * i] * sa, OA[2 * i + 1] * sa), 0xfffffffeu);
        auto s0 = __builtin_amdgcn_permlane32_swap(w[0], w[2], false, false);
        auto s1 = __builtin_amdgcn_permlane32_swap(w[1], w[3], false, false);
        __builtin_amdgcn_raw_buffer_store_b128((u32x4){s0[0], s1[0], s0[1], s1[1]}, rsO, ooff, 0, 16);
#pragma unroll
        for (int i = 0; i < 4; ++i) w[i] = min(cvtpk(OB[2 * i] * sb, OB[2 * i + 1] * sb), 0xfffffffeu);
        auto t0 = __builtin_amdgcn_permlane32_swap(w[0], w[2], false, false);
        auto t1 = __builtin_amdgcn_permlane32_swap(w[1], w[3], false, false);
        __builtin_amdgcn_raw_buffer_store_b128((u32x4){t0[0], t1[0], t0[1], t1[1]}, rsO, ooff + 1024, 0, 16);
        if (wid >= 4) {
            if (lane == 0) __hip_atomic_fetch_add(sync + 1, 1u, __ATOMIC_RELAXED, __HIP_MEMORY_SCOPE_WORKGROUP);
            STAMP(4); STAMP_OUT(1);
            return;
        }
        asm volatile("s_waitcnt vmcnt(0)" ::: "memory");
        unsigned prev = 0;
        if (lane == 0) prev = __hip_atomic_fetch_add(sync, 1u, __ATOMIC_RELAXED, __HIP_MEMORY_SCOPE_WORKGROUP);
        prev = __builtin_amdgcn_readfirstlane(prev);
        if (prev == 3u && lane < 8)
            __hip_atomic_fetch_add((gu32*)(ws + WS_CNT + (qb * 8 + lane) * 256), 1u, __ATOMIC_RELAXED, __HIP_MEMORY_SCOPE_AGENT);
    }
    STAMP(4);
    const int bn = (grp & 1) * 4 + (idx >> 3), kq = wid;
    bf16x8 wf[16][2];
    {
        const char* Wp = ws + WS_WO + ((size_t)(kq * 16) * 16 + bn * 2) * 1024 + lane * 16;
#pragma unroll
        for (int k = 0; k < 16; ++k) { wf[k][0] = *(const bf16x8*)(Wp + k * 16384); wf[k][1] = *(const bf16x8*)(Wp + k * 16384 + 1024); }
    }
    f32x4* red = (f32x4*)(smem + A_RED);
#pragma unroll
    for (int ph = 0; ph < 2; ++ph) {
        const int row0 = qb * 512 + ph * 256 + (idx & 7) * 32;
        u32x4 of[16];
        const unsigned aoff = (unsigned)(kq * 16 * SEQ + row0 + r) * 32 + h * 16;
        if (ph == 0) {
            if (wid == 0) {
                gu32* cnt = (gu32*)(ws + WS_CNT + (qb * 8 + (idx & 7)) * 256);
                for (unsigned spins = 0; spins < (1u << 20); ++spins) {
                    if ((unsigned)__builtin_amdgcn_readfirstlane(__hip_atomic_load(cnt, __ATOMIC_RELAXED, __HIP_MEMORY_SCOPE_AGENT)) >= 64u) break;
                    __builtin_amdgcn_s_sleep(8);
                }
                if (lane == 0) __hip_atomic_store(sync + 2, 1u, __ATOMIC_RELAXED, __HIP_MEMORY_SCOPE_WORKGROUP);
            } else {
                for (unsigned spins = 0; spins < (1u << 24); ++spins) { if (lds_ld(sync + 2)) break; __builtin_amdgcn_s_sleep(8); }
            }
            asm volatile("" ::: "memory");
            STAMP(7);
#pragma unroll
            for (int k = 0; k < 16; ++k) of[k] = __builtin_amdgcn_raw_buffer_load_b128(rsO, aoff, k * SEQ * 32, 16);
        } else {
            for (unsigned spins = 0; spins < (1u << 24); ++spins) { if (lds_ld(sync + 1) >= 4u) break; __builtin_amdgcn_s_sleep(4); }
            asm volatile("" ::: "memory");
            STAMP(10);
            for (unsigned spins = 0; spins < (1u << 20); ++spins) {
                asm volatile("" ::: "memory");
#pragma unroll
                for (int k = 0; k < 16; ++k) of[k] = __builtin_amdgcn_raw_buffer_load_b128(rsO, aoff, k * SEQ * 32, 16);
                unsigned mx = 0;
#pragma unroll
                for (int k = 0; k < 16; ++k) mx = max(max(mx, max(of[k][0], of[k][1])), max(of[k][2], of[k][3]));
                if (!__any(mx == ~0u)) break;
            }
        }
        f32x16 acc[2] = {};
        SBAR();
#pragma unroll
        for (int k = 0; k < 16; ++k)
#pragma unroll
            for (int n = 0; n < 2; ++n) acc[n] = MFMA32(wf[k][n], __builtin_bit_cast(bf16x8, of[k]), acc[n]);
        if (ph == 0) STAMP(8); else STAMP(11);
        f32x4 own[2];
#pragma unroll
        for (int d = 0; d < 4; ++d)
#pragma unroll
            for (int g = 0; g < 2; ++g) {
                const int e = 4 * (2 * (d & 1) + g);
                const f32x4 v = {acc[d >> 1][e], acc[d >> 1][e + 1], acc[d >> 1][e + 2], acc[d >> 1][e + 3]};
                if (d == kq) own[g] = v;
                else red[((d * 3 + ((kq - d - 1) & 3)) * 2 + g) * 64 + lane] = v;
            }
        lds_arrive_wait(sync + 4 + 2 * ph, 4u, lane);
#pragma unroll
        for (int sl = 0; sl < 3; ++sl)
#pragma unroll
            for (int g = 0; g < 2; ++g) own[g] += red[((kq * 3 + sl) * 2 + g) * 64 + lane];
        auto s0 = __builtin_amdgcn_permlane32_swap(cvtpk(own[0][0], own[0][1]), cvtpk(own[1][0], own[1][1]), false, false);
        auto s1 = __builtin_amdgcn_permlane32_swap(cvtpk(own[0][2], own[0][3]), cvtpk(own[1][2], own[1][3]), false, false);
        *(u32x4*)(out + (size_t)(row0 + r) * DM + bn * 64 + kq * 16 + h * 8) = (u32x4){s0[0], s1[0], s0[1], s1[1]};
        if (ph == 0) { lds_arrive_wait(sync + 5, 4u, lane); STAMP(9); }
    }
    STAMP(12); STAMP_OUT(1);
}

extern "C" void kernel_launch(void* const* d_in, const int* in_sizes, int n_in,
                              void* d_out, int out_size, void* d_ws, size_t ws_size,
                              hipStream_t stream) {
    const u16* x = (const u16*)d_in[0];
    const u16* wq = (const u16*)d_in[1];
    const u16* wk = (const u16*)d_in[2];
    const u16* wv = (const u16*)d_in[3];
    const u16* woA = (const u16*)d_in[4];
    const u16* wqkv = (const u16*)d_in[6];
    const u16* woB = (const u16*)d_in[7];
    const u16* gw = (const u16*)d_in[8];
    char* ws = (char*)d_ws;
    (void)hipFuncSetAttribute((const void*)k_proj, hipFuncAttributeMaxDynamicSharedMemorySize, P_LDS);
    (void)hipFuncSetAttribute((const void*)k_attn, hipFuncAttributeMaxDynamicSharedMemorySize, A_LDS);
    k_proj<<<256, 512, P_LDS, stream>>>(x, wq, wk, wv, wqkv, woA, woB, gw, ws);
    k_attn<<<256, 512, A_LDS, stream>>>(ws, (u16*)d_out);
}
```

```cpp
#include <hip/hip_runtime.h>
#include <hip/hip_bf16.h>
#include <stdint.h>

typedef __attribute__((ext_vector_type(8))) __bf16 bf16x8;
typedef __attribute__((ext_vector_type(16))) __bf16 bf16x16;
typedef __attribute__((ext_vector_type(4))) short s16x4;
typedef __attribute__((ext_vector_type(8))) short s16x8;
typedef __attribute__((ext_vector_type(4))) float f32x4;
typedef __attribute__((ext_vector_type(8))) float f32x8;
typedef __attribute__((ext_vector_type(16))) float f32x16;
typedef __attribute__((ext_vector_type(4))) unsigned u32x4;
typedef __attribute__((ext_vector_type(2))) unsigned u32x2;
typedef unsigned short u16;
typedef __attribute__((address_space(3))) const char* lds_cptr;
typedef __attribute__((address_space(3))) char* lds_ptr;

constexpr int SEQ = 2048, DM = 512, NH = 32, DH = 16;
constexpr float QSCALE = 0.25f * 1.4426950408889634f;

constexpr int VBLK = 544;
constexpr size_t KV_TILE = 1024 + 2 * VBLK;
constexpr size_t KV_HEAD = 64 * KV_TILE;
constexpr size_t WS_Q = 0;
constexpr size_t WS_O = WS_Q + (size_t)64 * SEQ * DH * 2;
constexpr size_t WS_WO = WS_O + (size_t)64 * SEQ * DH * 2;
constexpr size_t WS_G = WS_WO + (size_t)1024 * 512 * 2;
constexpr size_t WS_CNT = WS_G + 2 * SEQ * 4;
constexpr size_t WS_KV = WS_CNT + 64 * 256;
constexpr size_t WS_END = WS_KV + 34 * KV_HEAD;

#define SBAR() __builtin_amdgcn_sched_barrier(0)
#define MFMA32(a, b, c) __builtin_amdgcn_mfma_f32_32x32x16_bf16(a, b, c, 0, 0, 0)
#define MFMA16(a, b, c) __builtin_amdgcn_mfma_f32_16x16x32_bf16(a, b, c, 0, 0, 0)

typedef __attribute__((ext_vector_type(2))) __bf16 bf16x2;
typedef __attribute__((ext_vector_type(2))) float f32x2;
__device__ __forceinline__ unsigned cvtpk(float lo, float hi) {
    return __builtin_bit_cast(unsigned, __builtin_convertvector((f32x2){lo, hi}, bf16x2));
}
#define STAMP(i)
#define STAMP_DECL
#define STAMP_OUT(k)
__device__ __forceinline__ float bf2f(u16 v) { return __uint_as_float((unsigned)v << 16); }
__device__ __forceinline__ float rbf(float v) { return __uint_as_float(cvtpk(v, 0.f) << 16); }

__device__ __forceinline__ void glds16(const void* g, unsigned lds_base) {
    unsigned sv;
    asm volatile("s_mov_b32 %0, m0\n\ts_mov_b32 m0, %2\n\ts_nop 0\n\tglobal_load_lds_dwordx4 %1, off\n\ts_mov_b32 m0, %0"
                 : "=&s"(sv) : "v"(g), "s"(lds_base) : "memory");
}
__device__ __forceinline__ s16x4 ldtr(lds_cptr p) {
    typedef short v4i16_t __attribute__((ext_vector_type(4)));
    return __builtin_bit_cast(s16x4, __builtin_amdgcn_ds_read_tr16_b64_v4i16((__attribute__((address_space(3))) v4i16_t*)p));
}
__device__ __forceinline__ bf16x8 ld128(lds_cptr p) { return *(const __attribute__((address_space(3))) bf16x8*)p; }
__device__ __forceinline__ bf16x8 cat8(s16x4 a, s16x4 b) {
    s16x8 v = {a[0], a[1], a[2], a[3], b[0], b[1], b[2], b[3]}; return __builtin_bit_cast(bf16x8, v);
}
#define ST16_PLAIN(p, v) (*(u32x4*)(p) = (v))
#define ST16_NT(p, v) __builtin_nontemporal_store((u32x4)(v), (u32x4*)(p))
#define ST16_SC1(p, v) do { u32x4 v_ = (v); asm volatile("global_store_dwordx4 %0, %1, off sc1\n\ts_nop 1" :: "v"(p), "v"(v_) : "memory"); } while (0)
#define ST16A ST16_NT
#define ST16B ST16_PLAIN
#define WAITV_BAR(N) asm volatile("s_waitcnt vmcnt(" #N ") lgkmcnt(0)\n\ts_barrier" ::: "memory")

constexpr int P_STAGE = 32768, P_NST = 4, P_EXTRA = P_NST * P_STAGE, P_GW = P_EXTRA + 16384, P_WO = P_GW + 1024, P_LDS = P_WO + 4096;

__device__ __forceinline__ unsigned wswz(unsigned row) { return ((row & 3) << 2) | ((row >> 2) & 3); }
#define LBAR() asm volatile("s_waitcnt lgkmcnt(0)\n\ts_barrier" ::: "memory")

#define TOUCH_KC 2
template <bool VT>
__device__ __forceinline__ void proj_loop(f32x16 (&acc)[2][2], lds_cptr L, const unsigned (&xoff)[2][4], const unsigned (&woff)[2][2],
                                          bool do_touch, unsigned& touch, const char* dst, int dm, int dc, int du) {
    bf16x8 xf[2][2], wf[2][2];
#define PMM(m, c, buf) acc[m][c] = VT ? MFMA32(xf[buf][m], wf[buf][c], acc[m][c]) : MFMA32(wf[buf][c], xf[buf][m], acc[m][c])
#define PRD(S, s, buf, i) do { xf[buf][i] = ld128(S + xoff[i][s]); wf[buf][i] = cat8(ldtr(S + woff[i][0] + (s) * 4096), ldtr(S + woff[i][1] + (s) * 4096)); } while (0)
    LBAR();
    PRD(L, 0, 0, 0); PRD(L, 0, 0, 1);
#pragma unroll 1
    for (int kc = 0; kc < 8; ++kc) {
        lds_cptr S = L + (kc & 3) * P_STAGE;
        lds_cptr SN = L + ((kc + 1) & 3) * P_STAGE;
        if (kc == TOUCH_KC && do_touch) {
#pragma unroll
            for (int i = 0; i < 8; ++i)
                asm volatile("global_load_dword %0, %1, off" : "+v"(touch) : "v"(dst + (i >> 2) * dm + ((i >> 1) & 1) * dc + (i & 1) * du) : "memory");
        }
#pragma unroll
        for (int s = 0; s < 4; ++s) {
            SBAR();
            if (s == 3 && kc < 7) LBAR();
            PMM(0, 0, s & 1);
            SBAR();
            if (s < 3) PRD(S, s + 1, (s + 1) & 1, 0); else PRD(SN, 0, 0, 0);
            SBAR();
            PMM(0, 1, s & 1);
            SBAR();
            if (s < 3) PRD(S, s + 1, (s + 1) & 1, 1); else PRD(SN, 0, 0, 1);
            SBAR();
            PMM(1, 0, s & 1);
            PMM(1, 1, s & 1);
        }
    }
#undef PMM
#undef PRD
}

__global__ __launch_bounds__(640) void k_proj(const u16* __restrict__ x, const u16* __restrict__ wq, const u16* __restrict__ wk,
                                                 const u16* __restrict__ wv, const u16* __restrict__ wqkv, const u16* __restrict__ woA,
                                                 const u16* __restrict__ woB, const u16* __restrict__ gw, char* __restrict__ ws) {
    extern __shared__ __attribute__((aligned(16))) char smem[];
    const int tid = threadIdx.x, lane = tid & 63, wid = __builtin_amdgcn_readfirstlane(tid >> 6);
    const int bid = blockIdx.x, grp = bid & 7, idx = bid >> 3;
    const int cj = idx >> 2;
    const int bi = (grp >> 1) * 4 + (idx & 3);
    const int bj = (cj >> 1) * 4 + (grp & 1) * 2 + (cj & 1);
    const unsigned lds0 = (unsigned)(uintptr_t)smem;
    lds_cptr L = (lds_cptr)smem;
    char* kvbase = ws + WS_KV;
    const u32x4 ones = {0x3f803f80u, 0x3f803f80u, 0x3f803f80u, 0x3f803f80u};
    STAMP_DECL; STAMP(0);
    if (wid >= 8) {
        const int erow = (bj >> 1) * 16 + (lane & 15), eg = lane >> 4;
        const unsigned ex0 = erow * 128 + 16 * (eg ^ ((erow >> 1) & 7)), ex1 = erow * 128 + 16 * ((4 + eg) ^ ((erow >> 1) & 7));
        lds_cptr ewp = L + P_EXTRA + (8 * eg + ((lane & 15) >> 2)) * 32 + 8 * (lane & 3);
        lds_cptr gwp = L + P_GW + eg * 16;
        f32x4 eacc = {};
        __builtin_amdgcn_s_setprio(3);
#pragma unroll 1
        for (int kc = 0; kc < 8; ++kc) {
            LBAR();
            lds_cptr S = L + (kc & 3) * P_STAGE;
            if (wid == 8) {
                eacc = MFMA16(ld128(S + ex0), cat8(ldtr(ewp + kc * 2048), ldtr(ewp + kc * 2048 + 128)), eacc);
                eacc = MFMA16(ld128(S + ex1), cat8(ldtr(ewp + kc * 2048 + 1024), ldtr(ewp + kc * 2048 + 1152)), eacc);
            } else {
                eacc = MFMA16(ld128(S + ex0), ld128(gwp + kc * 128), eacc);
                eacc = MFMA16(ld128(S + ex1), ld128(gwp + kc * 128 + 64), eacc);
            }
        }
        const int key0 = bi * 128 + (bj >> 1) * 16;
        if (wid == 8) {
            const unsigned e0 = cvtpk(eacc[0], eacc[1]), e1 = cvtpk(eacc[2], eacc[3]);
            char* tb = kvbase + (size_t)(key0 >> 5) * KV_TILE;
            const int u = (key0 >> 4) & 1, d = lane & 15;
            if (bj & 1) {
                *(u32x2*)(tb + 1024 + u * VBLK + (eg >> 1) * 256 + d * 16 + (eg & 1) * 8) = (u32x2){e0, e1};
                if (lane < 2) *(u32x4*)(tb + 1024 + u * VBLK + 512 + lane * 16) = lane ? (u32x4){0u, 0u, 0u, 0u} : ones;
            } else {
                u16* kb = (u16*)(tb + (d >> 3) * 512 + (u * 16 + 4 * eg) * 16 + (d & 7) * 2);
                kb[0] = (u16)e0; kb[8] = (u16)(e0 >> 16); kb[16] = (u16)e1; kb[24] = (u16)(e1 >> 16);
            }
        } else {
            if (!(bj & 1) && (lane & 15) == 0) {
                f32x4 g, og;
#pragma unroll
                for (int i = 0; i < 4; ++i) { g[i] = rbf(__builtin_amdgcn_rcpf(1.f + __expf(-rbf(eacc[i])))); og[i] = rbf(1.f - g[i]); }
                float* G = (float*)(ws + WS_G);
                *(f32x4*)(G + key0 + 4 * eg) = g; *(f32x4*)(G + SEQ + key0 + 4 * eg) = og;
            }
            if (bid == 0) *(unsigned*)(ws + WS_CNT + lane * 256) = 0u;
        }
        STAMP(6); STAMP_OUT(0);
        return;
    }
    if (wid >= 4) {
        const int lw = wid - 4;
        const u16* W; int ldw, col0;
        if (bj < 4) { W = wq; ldw = 512; col0 = bj * 128; } else { W = wqkv; ldw = 1536; col0 = (bj - 4) * 128; }
        const u16* xs; const u16* wsrc;
        {
            const int row = 8 * lw + (lane >> 3), ch = (lane & 7) ^ ((row >> 1) & 7);
            xs = x + (size_t)(bi * 128 + row) * DM + ch * 8;
            const int kr = 4 * lw + (lane >> 4), wch = (lane & 15) ^ wswz(kr);
            wsrc = W + (size_t)kr * ldw + col0 + wch * 8;
        }
#define PSTAGE(s) do { const unsigned base_ = lds0 + ((s) & (P_NST - 1)) * P_STAGE + lw * 1024; \
        _Pragma("unroll") for (int i = 0; i < 4; ++i) { glds16(xs + (s) * 64 + i * 32 * DM, base_ + i * 4096); glds16(wsrc + ((s) * 64 + i * 16) * ldw, base_ + 16384 + i * 4096); } } while (0)
        PSTAGE(0);
        {
            const u16* we = (bj & 1) ? wv : wk;
#pragma unroll
            for (int i = 0; i < 4; ++i) glds16(we + (lw + 4 * i) * 512 + lane * 8, lds0 + P_EXTRA + (lw + 4 * i) * 1024);
            glds16(gw + lane * 8, lds0 + P_GW);
        }
        PSTAGE(1);
        STAMP(4);
        WAITV_BAR(8);
        PSTAGE(2);
#pragma unroll 1
        for (int kc = 1; kc < 6; ++kc) {
            WAITV_BAR(8);
            PSTAGE(kc + 2);
        }
        {
            const int ks = bid >> 2, kr = 4 * lw + (lane >> 4);
            const u16* wo = (ks < 32) ? woA + (size_t)ks * 16 * DM : woB + (size_t)(ks - 32) * 16 * DM;
            glds16(wo + (size_t)kr * DM + (bid & 3) * 128 + ((lane & 15) ^ wswz(kr)) * 8, lds0 + P_WO + lw * 1024);
        }
        WAITV_BAR(9);
        WAITV_BAR(0);
        {
            const int h = lane >> 5, blk = (lane >> 4) & 1, q = (lane & 15) >> 2, p = lane & 3;
            const int kr0 = 8 * h + q, kr1 = kr0 + 4, ch = 4 * lw + 2 * blk + (p >> 1);
            lds_cptr wb = L + P_WO + 8 * (p & 1);
            *(bf16x8*)(ws + WS_WO + ((size_t)((bid >> 2) * 16 + (bid & 3) * 4 + lw) * 64 + lane) * 16) =
                cat8(ldtr(wb + 256 * kr0 + 16 * (ch ^ wswz(kr0))), ldtr(wb + 256 * kr1 + 16 * (ch ^ wswz(kr1))));
        }
        STAMP(5); STAMP_OUT(0);
        return;
    }
    const int wr = wid >> 1, wc = wid & 1, r = lane & 31, h = lane >> 5;
    f32x16 acc[2][2] = {};
    unsigned xoff[2][4], woff[2][2];
#pragma unroll
    for (int m = 0; m < 2; ++m)
#pragma unroll
        for (int s = 0; s < 4; ++s) { const int row = wr * 64 + m * 32 + r; xoff[m][s] = row * 128 + 16 * ((2 * s + h) ^ ((row >> 1) & 7)); }
    {
        const int blk = (lane >> 4) & 1, q = (lane & 15) >> 2, p = lane & 3;
#pragma unroll
        for (int t = 0; t < 2; ++t) {
            const int krow = 8 * h + 4 * t + q;
#pragma unroll
            for (int c = 0; c < 2; ++c)
                woff[c][t] = 16384 + 256 * krow + 16 * ((4 * (wc * 2 + c) + 2 * blk + (p >> 1)) ^ wswz(krow)) + 8 * (p & 1);
        }
    }
    char* dst; int dm, dc, du;
    {
        const int srow0 = bi * 128 + wr * 64, hd0 = (bj & 3) * 8 + wc * 4;
        if (bj < 8) {
            dst = ws + WS_Q + (size_t)(((bj >> 2) * 32 + hd0) * SEQ + srow0) * 32 + lane * 16;
            dm = 32 * 32; du = SEQ * 32; dc = 2 * du;
        } else if (bj < 12) {
            dst = kvbase + (size_t)(1 + hd0) * KV_HEAD + (srow0 >> 5) * KV_TILE + h * 512 + r * 16;
            dm = (int)KV_TILE; du = (int)KV_HEAD; dc = 2 * du;
        } else {
            dst = kvbase + (size_t)(1 + hd0 + (r >> 4)) * KV_HEAD + (srow0 >> 5) * KV_TILE + 1024 + h * 256 + (r & 15) * 16;
            dm = (int)KV_TILE; du = VBLK; dc = 2 * (int)KV_HEAD;
        }
    }
    unsigned touch = 0;
    const bool do_touch = (bj < 8) ? (lane & 7) == 0 : ((idx & 3) == 0 && (lane & 3) == 0);
    STAMP(1);
    if (bj >= 12) proj_loop<true>(acc, L, xoff, woff, do_touch, touch, dst, dm, dc, du);
    else proj_loop<false>(acc, L, xoff, woff, do_touch, touch, dst, dm, dc, du);
    STAMP(2);
    asm volatile("s_waitcnt vmcnt(0)" : "+v"(touch) :: "memory");
    const float sc = (bj < 8) ? QSCALE : 1.f;
#pragma unroll
    for (int m = 0; m < 2; ++m)
#pragma unroll
        for (int c = 0; c < 2; ++c) {
            const f32x16 a = acc[m][c];
            unsigned w[8];
#pragma unroll
            for (int i = 0; i < 8; ++i) w[i] = cvtpk(a[2 * i] * sc, a[2 * i + 1] * sc);
#pragma unroll
            for (int u = 0; u < 2; ++u) {
                auto s0 = __builtin_amdgcn_permlane32_swap(w[4 * u + 0], w[4 * u + 2], false, false);
                auto s1 = __builtin_amdgcn_permlane32_swap(w[4 * u + 1], w[4 * u + 3], false, false);
                ST16A(dst + m * dm + c * dc + u * du, ((u32x4){s0[0], s1[0], s0[1], s1[1]}));
            }
        }
    if (bj >= 12 && (r & 15) < 2) {
        char* vb = dst - h * 256 - (r & 15) * 16 + h * VBLK + 512 + (r & 1) * 16;
        const u32x4 cv = (r & 1) ? (u32x4){0u, 0u, 0u, 0u} : ones;
#pragma unroll
        for (int m = 0; m < 2; ++m)
#pragma unroll
            for (int c = 0; c < 2; ++c) *(u32x4*)(vb + m * dm + c * dc) = cv;
    }
    STAMP(3); STAMP_OUT(0);
}

constexpr int A_RED = (int)KV_HEAD;
constexpr int A_GATE = A_RED + 24576, A_SYNC = A_GATE + 2048, A_LDS = A_SYNC + 64;

#define PIN(x) asm volatile("" : "+v"(x))
#define SMF(acc, vf, p) do { acc = __builtin_amdgcn_smfmac_f32_32x32x32_bf16(vf, __builtin_bit_cast(bf16x16, p), acc, sidx, 0, 0); PIN(acc); } while (0)
#define EXPR(S, lo, hi) _Pragma("unroll") for (int e_ = lo; e_ < hi; ++e_) S[e_] = __builtin_amdgcn_exp2f(S[e_])
#define LO8(S) __builtin_shufflevector(S, S, 0, 1, 2, 3, 4, 5, 6, 7)
#define HI8(S) __builtin_shufflevector(S, S, 8, 9, 10, 11, 12, 13, 14, 15)

__device__ __forceinline__ float max16(const f32x16& s) {
    float a = fmaxf(fmaxf(s[0], s[1]), s[2]), b = fmaxf(fmaxf(s[3], s[4]), s[5]);
    a = fmaxf(fmaxf(a, s[6]), s[7]); b = fmaxf(fmaxf(b, s[8]), s[9]);
    a = fmaxf(fmaxf(a, s[10]), s[11]); b = fmaxf(fmaxf(b, s[12]), s[13]);
    a = fmaxf(fmaxf(a, s[14]), s[15]);
    return fmaxf(a, b);
}
__device__ __forceinline__ float xhalf_max(float m) {
    auto rr = __builtin_amdgcn_permlane32_swap(__float_as_uint(m), __float_as_uint(m), false, false);
    return fmaxf(__uint_as_float(rr[0]), __uint_as_float(rr[1]));
}

#define ATT_STEP(KN, V1C, V0N) do { \
    SBAR(); EXPR(SA, 0, 2);   SBAR(); SMF(OB, v1, HI8(SB)); v1 = ld128(V1C); \
    SBAR(); EXPR(SA, 2, 6);   SBAR(); SB = MFMA32(kf, qbf, CB); PIN(SB); kf = ld128(KN); \
    SBAR(); EXPR(SA, 6, 10);  SBAR(); SMF(OA, v0, LO8(SA)); \
    SBAR(); EXPR(SA, 10, 16); EXPR(SB, 0, 2); SBAR(); SMF(OA, v1, HI8(SA)); \
    SBAR(); EXPR(SB, 2, 6);   SBAR(); SA = MFMA32(kf, qa, CA); PIN(SA); \
    SBAR(); EXPR(SB, 6, 10);  SBAR(); SMF(OB, v0, LO8(SB)); v0 = ld128(V0N); \
    SBAR(); EXPR(SB, 10, 16); } while (0)

#define POLL_SLEEP 4
#define LATE_DELAY 0
#define PRIO_C 5
typedef __attribute__((address_space(1))) unsigned gu32;
typedef __attribute__((address_space(3))) unsigned lu32;
__device__ __forceinline__ unsigned lds_ld(lu32* p) { return __builtin_amdgcn_readfirstlane(__hip_atomic_load(p, __ATOMIC_RELAXED, __HIP_MEMORY_SCOPE_WORKGROUP)); }
__device__ __forceinline__ void lds_arrive_wait(lu32* w, unsigned target, int lane) {
    asm volatile("s_waitcnt lgkmcnt(0)" ::: "memory");
    if (lane == 0) __hip_atomic_fetch_add(w, 1u, __ATOMIC_RELAXED, __HIP_MEMORY_SCOPE_WORKGROUP);
    for (unsigned spins = 0; spins < (1u << 22); ++spins) { if (lds_ld(w) >= target) break; __builtin_amdgcn_s_sleep(1); }
    asm volatile("" ::: "memory");
}

__global__ __launch_bounds__(512, 2) void k_attn(char* __restrict__ ws, u16* __restrict__ out) {
    extern __shared__ __attribute__((aligned(16))) char smem[];
    const int tid = threadIdx.x, lane = tid & 63, wid = __builtin_amdgcn_readfirstlane(tid >> 6);
    const int bid = blockIdx.x, grp = bid & 7, idx = bid >> 3;
    const int head = (idx >> 4) * 32 + (grp & 1) * 16 + (idx & 15), qb = grp >> 1;
    const int kvh = head < 32 ? 0 : head - 31;
    const unsigned lds0 = (unsigned)(uintptr_t)smem;
    const int q0 = qb * 512 + wid * 64;
    STAMP_DECL; STAMP(0);
    const int r = lane & 31, h = lane >> 5;
    bf16x8 qa, qbf;
    {
        const char* qsrc = ws + WS_Q + (size_t)(head * SEQ + q0) * 32 + lane * 16;
        asm volatile("global_load_dwordx4 %0, %2, off\n\tglobal_load_dwordx4 %1, %2, off offset:1024"
                     : "=&v"(qa), "=&v"(qbf) : "v"(qsrc) : "memory");
    }
    const char* ksrc = ws + WS_KV + (size_t)kvh * KV_HEAD + lane * 16;
#define KVDMA(p) do { int p_ = qb * 33 + (p); p_ = p_ >= 132 ? p_ - 132 : p_; glds16(ksrc + p_ * 1024, lds0 + (p) * 1024); } while (0)
    KVDMA(wid);
    STAMP(14);
    const __amdgpu_buffer_rsrc_t rsO = __builtin_amdgcn_make_buffer_rsrc((void*)(ws + WS_O), 0, 64 * SEQ * DH * 2, 0x00020000);
    const unsigned ooff = (unsigned)(head * SEQ + q0) * 32 + lane * 16;
    const u32x4 poison = {~0u, ~0u, ~0u, ~0u};
    lds_cptr L = (lds_cptr)smem;
    lu32* sync = (lu32*)(smem + A_SYNC);
    if (tid < 16) sync[tid] = 0u;
    lds_cptr kp = L + lane * 16;
    lds_cptr vp = L + 1024 + (r < 16 ? h * 256 + r * 16 : ((r == 16 || r == 20) ? 512 : 528));
    int sidx = 0xDDDD; PIN(sidx);
    f32x16 OA, OB, CA, CB, SA, SB;
#pragma unroll
    for (int i = 0; i < 16; ++i) { OA[i] = 0.f; OB[i] = 0.f; SB[i] = 0.f; }
    asm volatile("" : "+v"(OA), "+v"(OB), "+v"(SB));
    asm volatile("s_waitcnt vmcnt(0) lgkmcnt(0)\n\ts_barrier" : "+v"(qa), "+v"(qbf) :: "memory");
#define LANDED(w) do { if (lane == 0) __hip_atomic_fetch_add(sync + 8 + (w), 1u, __ATOMIC_RELAXED, __HIP_MEMORY_SCOPE_WORKGROUP); } while (0)
#define AWAIT(w) do { SBAR(); for (unsigned sp_ = 0; sp_ < (1u << 22); ++sp_) { if (lds_ld(sync + 8 + (w)) >= 4u) break; __builtin_amdgcn_s_sleep(1); } \
                      asm volatile("" ::: "memory"); SBAR(); } while (0)
    if (wid >= 4) {
#pragma unroll 1
        for (int i = 0; i < 8; ++i) KVDMA(4 + wid + 4 * i);
        asm volatile("s_waitcnt vmcnt(4)" ::: "memory"); LANDED(0);
#pragma unroll 1
        for (int i = 8; i < 16; ++i) KVDMA(4 + wid + 4 * i);
        asm volatile("s_waitcnt vmcnt(8)" ::: "memory"); LANDED(1);
#pragma unroll 1
        for (int i = 16; i < 24; ++i) KVDMA(4 + wid + 4 * i);
        asm volatile("s_waitcnt vmcnt(8)" ::: "memory"); LANDED(2);
#pragma unroll 1
        for (int i = 24; i < 31; ++i) KVDMA(4 + wid + 4 * i);
        if (wid < 6) glds16(ws + WS_G + ((head < 32 ? 0 : SEQ) + qb * 512) * 4 + (wid - 4) * 1024 + lane * 16, lds0 + A_GATE + (wid - 4) * 1024);
        __builtin_amdgcn_raw_buffer_store_b128(poison, rsO, ooff - 8192, 0, 16); __builtin_amdgcn_raw_buffer_store_b128(poison, rsO, ooff - 7168, 0, 16);
        __builtin_amdgcn_raw_buffer_store_b128(poison, rsO, ooff, 0, 16); __builtin_amdgcn_raw_buffer_store_b128(poison, rsO, ooff + 1024, 0, 16);
        asm volatile("s_waitcnt vmcnt(0)" ::: "memory");
        unsigned prev = 0;
        if (lane == 0) prev = __hip_atomic_fetch_add(sync + 8 + 3, 1u, __ATOMIC_RELAXED, __HIP_MEMORY_SCOPE_WORKGROUP);
        if (__builtin_amdgcn_readfirstlane(prev) == 3u && lane < 8)
            __hip_atomic_fetch_add((gu32*)(ws + WS_CNT + (qb * 8 + lane) * 256), 1u, __ATOMIC_RELAXED, __HIP_MEMORY_SCOPE_AGENT);
        AWAIT(3);
        __builtin_amdgcn_s_setprio(1);
    }
    STAMP(1);
    float ma, mb;
    bf16x8 kf, v0, v1;
    {
        const f32x16 z = {};
        kf = ld128(kp);
        const bf16x8 k1 = ld128(kp + KV_TILE), k2 = ld128(kp + 2 * KV_TILE), k3 = ld128(kp + 3 * KV_TILE);
        v0 = ld128(vp); v1 = ld128(vp + VBLK);
        const f32x16 s0 = MFMA32(kf, qa, z), s1 = MFMA32(k1, qa, z), s2 = MFMA32(kf, qbf, z), s3 = MFMA32(k1, qbf, z);
        ma = fmaxf(max16(s0), max16(s1)); mb = fmaxf(max16(s2), max16(s3));
        const f32x16 s4 = MFMA32(k2, qa, z), s5 = MFMA32(k3, qa, z), s6 = MFMA32(k2, qbf, z), s7 = MFMA32(k3, qbf, z);
        ma = xhalf_max(fmaxf(ma, fmaxf(max16(s4), max16(s5))));
        mb = xhalf_max(fmaxf(mb, fmaxf(max16(s6), max16(s7))));
    }
    for (int attempt = 0; attempt < 2; ++attempt) {
#pragma unroll
        for (int i = 0; i < 16; ++i) { CA[i] = -ma; CB[i] = -mb; }
        asm volatile("" : "+v"(CA), "+v"(CB));
        SA = MFMA32(kf, qa, CA);
        int c0 = 0;
        if (attempt == 0 && wid < 4) {
            STAMP(2);
            ATT_STEP(kp + 1 * KV_TILE, vp + 0 * KV_TILE + VBLK, vp + 1 * KV_TILE);
            ATT_STEP(kp + 2 * KV_TILE, vp + 1 * KV_TILE + VBLK, vp + 2 * KV_TILE);
            ATT_STEP(kp + 3 * KV_TILE, vp + 2 * KV_TILE + VBLK, vp + 3 * KV_TILE);
            AWAIT(0);
            ATT_STEP(kp + 4 * KV_TILE, vp + 3 * KV_TILE + VBLK, vp + 4 * KV_TILE);
            ATT_STEP(kp + 5 * KV_TILE, vp + 4 * KV_TILE + VBLK, vp + 5 * KV_TILE);
            ATT_STEP(kp + 6 * KV_TILE, vp + 5 * KV_TILE + VBLK, vp + 6 * KV_TILE);
            ATT_STEP(kp + 7 * KV_TILE, vp + 6 * KV_TILE + VBLK, vp + 7 * KV_TILE);
            ATT_STEP(kp + 8 * KV_TILE, vp + 7 * KV_TILE + VBLK, vp + 8 * KV_TILE);
            AWAIT(1);
            STAMP(6);
            c0 = 1;
        }
#pragma unroll 1
        for (int c = c0; c < 8; ++c) {
            if (c == PRIO_C) __builtin_amdgcn_s_setprio(0);
            if (c == 2) AWAIT(2);
            if (c == 4) AWAIT(3);
            lds_cptr kc = kp + c * 8 * KV_TILE;
            lds_cptr vc = vp + c * 8 * KV_TILE;
#pragma unroll
            for (int t = 0; t < 8; ++t) ATT_STEP(kc + (t + 1) * KV_TILE, vc + t * KV_TILE + VBLK, vc + (t + 1) * KV_TILE);
        }
        SBAR();
        SMF(OB, v1, HI8(SB));
        if (attempt == 0) STAMP(3); else STAMP(5);
        const float la = OA[8], lb = OB[8];
        float oa = 0.f, ob = 0.f;
#pragma unroll
        for (int i = 0; i < 8; ++i) { oa += fabsf(OA[i]); ob += fabsf(OB[i]); }
        const bool ok = (la <= 0x1p125f) && (lb <= 0x1p125f) && (oa <= 0x1p126f) && (ob <= 0x1p126f);
        if (attempt == 1 || __all(ok)) break;
        const f32x16 z = {};
        float ta = -INFINITY, tb = -INFINITY;
#pragma unroll 1
        for (int t = 0; t < 64; ++t) {
            const bf16x8 k0 = ld128(kp + t * KV_TILE);
            ta = fmaxf(ta, max16(MFMA32(k0, qa, z)));
            tb = fmaxf(tb, max16(MFMA32(k0, qbf, z)));
        }
        ma = xhalf_max(ta); mb = xhalf_max(tb);
#pragma unroll
        for (int i = 0; i < 16; ++i) { OA[i] = 0.f; OB[i] = 0.f; SB[i] = 0.f; }
        kf = ld128(kp); v0 = ld128(vp); v1 = ld128(vp + VBLK);
    }
    {
        const float* gl = (const float*)(smem + A_GATE) + wid * 64 + r;
        const float sa = gl[0] * __builtin_amdgcn_rcpf(OA[8]), sb = gl[32] * __builtin_amdgcn_rcpf(OB[8]);
        unsigned w[4];
#pragma unroll
        for (int i = 0; i < 4; ++i) w[i] = min(cvtpk(OA[2 * i] * sa, OA[2 * i + 1] * sa), 0xfffffffeu);
        auto s0 = __builtin_amdgcn_permlane32_swap(w[0], w[2], false, false);
        auto s1 = __builtin_amdgcn_permlane32_swap(w[1], w[3], false, false);
        __builtin_amdgcn_raw_buffer_store_b128((u32x4){s0[0], s1[0], s0[1], s1[1]}, rsO, ooff, 0, 16);
#pragma unroll
        for (int i = 0; i < 4; ++i) w[i] = min(cvtpk(OB[2 * i] * sb, OB[2 * i + 1] * sb), 0xfffffffeu);
        auto t0 = __builtin_amdgcn_permlane32_swap(w[0], w[2], false, false);
        auto t1 = __builtin_amdgcn_permlane32_swap(w[1], w[3], false, false);
        __builtin_amdgcn_raw_buffer_store_b128((u32x4){t0[0], t1[0], t0[1], t1[1]}, rsO, ooff + 1024, 0, 16);
        if (wid >= 4) {
            if (lane == 0) __hip_atomic_fetch_add(sync + 1, 1u, __ATOMIC_RELAXED, __HIP_MEMORY_SCOPE_WORKGROUP);
            STAMP(4); STAMP_OUT(1);
            return;
        }
    }
    STAMP(4);
    const int bn = (grp & 1) * 4 + (idx >> 3), kq = wid;
    bf16x8 wf[16][2];
    {
        const char* Wp = ws + WS_WO + ((size_t)(kq * 16) * 16 + bn * 2) * 1024 + lane * 16;
#pragma unroll
        for (int k = 0; k < 16; ++k) { wf[k][0] = *(const bf16x8*)(Wp + k * 16384); wf[k][1] = *(const bf16x8*)(Wp + k * 16384 + 1024); }
    }
    {
        gu32* cnt = (gu32*)(ws + WS_CNT + (qb * 8 + (idx & 7)) * 256);
        for (unsigned spins = 0; spins < (1u << 20); ++spins) {
            if ((unsigned)__builtin_amdgcn_readfirstlane(__hip_atomic_load(cnt, __ATOMIC_RELAXED, __HIP_MEMORY_SCOPE_AGENT)) >= 64u) break;
            __builtin_amdgcn_s_sleep(8);
        }
        asm volatile("" ::: "memory");
    }
    const unsigned long long ob = (unsigned long long)(uintptr_t)(ws + WS_O);
    const u32x4 srd = {(unsigned)ob, (unsigned)(ob >> 32) & 0xffffu, (unsigned)(64 * SEQ * DH * 2), 0x00020000u};
    f32x4* red = (f32x4*)(smem + A_RED);
#define OLD(k) asm volatile("s_bitcmp1_b32 %4, " #k "\n\ts_cbranch_scc0 1f\n\tbuffer_load_dwordx4 %0, %1, %2, %3 offen sc1\n1:" \
                            : "+v"(of[k]) : "v"(aoff), "s"(srd), "s"((k) * SEQ * 32), "s"(miss) : "memory", "scc")
#define OUSE(k) asm volatile("s_bitcmp1_b32 %2, " #k "\n\ts_cbranch_scc0 1f\n\t" \
                             "v_max3_u32 %3, %7, %8, %9\n\tv_max_u32 %3, %3, %10\n\tv_cmp_eq_u32 vcc, -1, %3\n\ts_cbranch_vccnz 1f\n\t" \
                             "v_mfma_f32_32x32x16_bf16 %0, %4, %6, %0\n\tv_mfma_f32_32x32x16_bf16 %1, %5, %6, %1\n\ts_bitset0_b32 %2, " #k "\n1:" \
                             : "+v"(acc[0]), "+v"(acc[1]), "+s"(miss), "=&v"(tmx) \
                             : "v"(wf[k][0]), "v"(wf[k][1]), "v"(of[k]), "v"(of[k][0]), "v"(of[k][1]), "v"(of[k][2]), "v"(of[k][3]) : "vcc", "scc")
#define OSTEP(k, n) asm volatile("s_waitcnt vmcnt(" #n ")" : "+v"(of[k]) :: "memory"); OUSE(k)
#define ALL16(M) M(0); M(1); M(2); M(3); M(4); M(5); M(6); M(7); M(8); M(9); M(10); M(11); M(12); M(13); M(14); M(15)
#pragma unroll 1
    for (int ph = 0; ph < 2; ++ph) {
        const int row0 = qb * 512 + ph * 256 + (idx & 7) * 32;
        u32x4 of[16];
        const unsigned aoff = (unsigned)(kq * 16 * SEQ + row0) * 32 + lane * 16;
        f32x16 acc[2] = {};
        if (ph == 1) {
            for (unsigned spins = 0; spins < (1u << 24); ++spins) { if (lds_ld(sync + 1) >= 4u) break; __builtin_amdgcn_s_sleep(4); }
            __builtin_amdgcn_s_sleep(LATE_DELAY);
        }
        if (ph == 0) STAMP(7); else STAMP(10);
        {
            unsigned miss = 0xffffu, tmx;
            asm volatile("s_waitcnt vmcnt(0)" : "+v"(acc[0]), "+v"(acc[1]) :: "memory");
            ALL16(OLD);
            OSTEP(0, 15); OSTEP(1, 14); OSTEP(2, 13); OSTEP(3, 12); OSTEP(4, 11); OSTEP(5, 10); OSTEP(6, 9); OSTEP(7, 8);
            OSTEP(8, 7); OSTEP(9, 6); OSTEP(10, 5); OSTEP(11, 4); OSTEP(12, 3); OSTEP(13, 2); OSTEP(14, 1); OSTEP(15, 0);
            for (unsigned spins = 0; miss && spins < (1u << 20); ++spins) {
                __builtin_amdgcn_s_sleep(POLL_SLEEP);
                ALL16(OLD);
                asm volatile("s_waitcnt vmcnt(0)" : "+v"(of[0]), "+v"(of[1]), "+v"(of[2]), "+v"(of[3]), "+v"(of[4]), "+v"(of[5]), "+v"(of[6]), "+v"(of[7]),
                             "+v"(of[8]), "+v"(of[9]), "+v"(of[10]), "+v"(of[11]), "+v"(of[12]), "+v"(of[13]), "+v"(of[14]), "+v"(of[15]) :: "memory");
                ALL16(OUSE);
            }
            asm volatile("s_nop 7\n\ts_nop 7" : "+v"(acc[0]), "+v"(acc[1]));
        }
        if (ph == 0) STAMP(8); else STAMP(11);
        f32x4 own[2];
#pragma unroll
        for (int d = 0; d < 4; ++d)
#pragma unroll
            for (int g = 0; g < 2; ++g) {
                const int e = 4 * (2 * (d & 1) + g);
                const f32x4 v = {acc[d >> 1][e], acc[d >> 1][e + 1], acc[d >> 1][e + 2], acc[d >> 1][e + 3]};
                if (d == kq) own[g] = v;
                else red[((d * 3 + ((kq - d - 1) & 3)) * 2 + g) * 64 + lane] = v;
            }
        if (ph == 0) lds_arrive_wait(sync + 4, 4u, lane);
        else LBAR();
#pragma unroll
        for (int sl = 0; sl < 3; ++sl)
#pragma unroll
            for (int g = 0; g < 2; ++g) own[g] += red[((kq * 3 + sl) * 2 + g) * 64 + lane];
        auto s0 = __builtin_amdgcn_permlane32_swap(cvtpk(own[0][0], own[0][1]), cvtpk(own[1][0], own[1][1]), false, false);
        auto s1 = __builtin_amdgcn_permlane32_swap(cvtpk(own[0][2], own[0][3]), cvtpk(own[1][2], own[1][3]), false, false);
        ST16B(out + (size_t)(row0 + r) * DM + bn * 64 + kq * 16 + h * 8, ((u32x4){s0[0], s1[0], s0[1], s1[1]}));
        if (ph == 0) { lds_arrive_wait(sync + 5, 4u, lane); STAMP(9); }
    }
    STAMP(12); STAMP_OUT(1);
}

extern "C" void kernel_launch(void* const* d_in, const int* in_sizes, int n_in,
                              void* d_out, int out_size, void* d_ws, size_t ws_size,
                              hipStream_t stream) {
    const u16* x = (const u16*)d_in[0];
    const u16* wq = (const u16*)d_in[1];
    const u16* wk = (const u16*)d_in[2];
    const u16* wv = (const u16*)d_in[3];
    const u16* woA = (const u16*)d_in[4];
    const u16* wqkv = (const u16*)d_in[6];
    const u16* woB = (const u16*)d_in[7];
    const u16* gw = (const u16*)d_in[8];
    char* ws = (char*)d_ws;
    (void)hipFuncSetAttribute((const void*)k_proj, hipFuncAttributeMaxDynamicSharedMemorySize, P_LDS);
    (void)hipFuncSetAttribute((const void*)k_attn, hipFuncAttributeMaxDynamicSharedMemorySize, A_LDS);
    k_proj<<<256, 640, P_LDS, stream>>>(x, wq, wk, wv, wqkv, woA, woB, gw, ws);
    k_attn<<<256, 512, A_LDS, stream>>>(ws, (u16*)d_out);
}
```

```cpp
#include <hip/hip_runtime.h>
#include <hip/hip_bf16.h>
#include <stdint.h>

typedef __attribute__((ext_vector_type(8))) __bf16 bf16x8;
typedef __attribute__((ext_vector_type(16))) __bf16 bf16x16;
typedef __attribute__((ext_vector_type(4))) short s16x4;
typedef __attribute__((ext_vector_type(8))) short s16x8;
typedef __attribute__((ext_vector_type(4))) float f32x4;
typedef __attribute__((ext_vector_type(8))) float f32x8;
typedef __attribute__((ext_vector_type(16))) float f32x16;
typedef __attribute__((ext_vector_type(4))) unsigned u32x4;
typedef __attribute__((ext_vector_type(2))) unsigned u32x2;
typedef unsigned short u16;
typedef __attribute__((address_space(3))) const char* lds_cptr;
typedef __attribute__((address_space(3))) char* lds_ptr;

constexpr int SEQ = 2048, DM = 512, NH = 32, DH = 16;
constexpr float QSCALE = 0.25f * 1.4426950408889634f;

constexpr int VBLK = 544;
constexpr size_t KV_TILE = 1024 + 2 * VBLK;
constexpr size_t KV_HEAD = 64 * KV_TILE;
constexpr size_t WS_Q = 0;
constexpr size_t WS_O = WS_Q + (size_t)64 * SEQ * DH * 2;
constexpr size_t WS_WO = WS_O + (size_t)64 * SEQ * DH * 2;
constexpr size_t WS_G = WS_WO + (size_t)1024 * 512 * 2;
constexpr size_t WS_CNT = WS_G + 2 * SEQ * 4;
constexpr size_t WS_KV = WS_CNT + 64 * 256;
constexpr size_t WS_END = WS_KV + 34 * KV_HEAD;

#define SBAR() __builtin_amdgcn_sched_barrier(0)
#define MFMA32(a, b, c) __builtin_amdgcn_mfma_f32_32x32x16_bf16(a, b, c, 0, 0, 0)
#define MFMA16(a, b, c) __builtin_amdgcn_mfma_f32_16x16x32_bf16(a, b, c, 0, 0, 0)

typedef __attribute__((ext_vector_type(2))) __bf16 bf16x2;
typedef __attribute__((ext_vector_type(2))) float f32x2;
__device__ __forceinline__ unsigned cvtpk(float lo, float hi) {
    return __builtin_bit_cast(unsigned, __builtin_convertvector((f32x2){lo, hi}, bf16x2));
}
#define STAMP(i)
#define STAMP_DECL
#define STAMP_OUT(k)
__device__ __forceinline__ float bf2f(u16 v) { return __uint_as_float((unsigned)v << 16); }
__device__ __forceinline__ float rbf(float v) { return __uint_as_float(cvtpk(v, 0.f) << 16); }

__device__ __forceinline__ void glds16(const void* g, unsigned lds_base) {
    unsigned sv;
    asm volatile("s_mov_b32 %0, m0\n\ts_mov_b32 m0, %2\n\ts_nop 0\n\tglobal_load_lds_dwordx4 %1, off\n\ts_mov_b32 m0, %0"
                 : "=&s"(sv) : "v"(g), "s"(lds_base) : "memory");
}
__device__ __forceinline__ s16x4 ldtr(lds_cptr p) {
    typedef short v4i16_t __attribute__((ext_vector_type(4)));
    return __builtin_bit_cast(s16x4, __builtin_amdgcn_ds_read_tr16_b64_v4i16((__attribute__((address_space(3))) v4i16_t*)p));
}
__device__ __forceinline__ bf16x8 ld128(lds_cptr p) { return *(const __attribute__((address_space(3))) bf16x8*)p; }
__device__ __forceinline__ bf16x8 cat8(s16x4 a, s16x4 b) {
    s16x8 v = {a[0], a[1], a[2], a[3], b[0], b[1], b[2], b[3]}; return __builtin_bit_cast(bf16x8, v);
}
#define ST16_PLAIN(p, v) (*(u32x4*)(p) = (v))
#define ST16_NT(p, v) __builtin_nontemporal_store((u32x4)(v), (u32x4*)(p))
#define ST16_SC1(p, v) do { u32x4 v_ = (v); asm volatile("global_store_dwordx4 %0, %1, off sc1\n\ts_nop 1" :: "v"(p), "v"(v_) : "memory"); } while (0)
#define ST16A ST16_NT
#define ST16B ST16_PLAIN
#define WAITV_BAR(N) asm volatile("s_waitcnt vmcnt(" #N ") lgkmcnt(0)\n\ts_barrier" ::: "memory")

constexpr int P_STAGE = 32768, P_NST = 4, P_EXTRA = P_NST * P_STAGE, P_GW = P_EXTRA + 16384, P_WO = P_GW + 1024, P_LDS = P_WO + 4096;

__device__ __forceinline__ unsigned wswz(unsigned row) { return ((row & 3) << 2) | ((row >> 2) & 3); }
#define LBAR() asm volatile("s_waitcnt lgkmcnt(0)\n\ts_barrier" ::: "memory")

#define TOUCH_KC 2
template <bool VT>
__device__ __forceinline__ void proj_loop(f32x16 (&acc)[2][2], lds_cptr L, const unsigned (&xoff)[2][4], const unsigned (&woff)[2][2],
                                          bool do_touch, unsigned& touch, const char* dst, int dm, int dc, int du) {
    bf16x8 xf[2][2], wf[2][2];
#define PMM(m, c, buf) acc[m][c] = VT ? MFMA32(xf[buf][m], wf[buf][c], acc[m][c]) : MFMA32(wf[buf][c], xf[buf][m], acc[m][c])
#define PRD(S, s, buf, i) do { xf[buf][i] = ld128(S + xoff[i][s]); wf[buf][i] = cat8(ldtr(S + woff[i][0] + (s) * 4096), ldtr(S + woff[i][1] + (s) * 4096)); } while (0)
    LBAR();
    PRD(L, 0, 0, 0); PRD(L, 0, 0, 1);
#pragma unroll 1
    for (int kc = 0; kc < 8; ++kc) {
        lds_cptr S = L + (kc & 3) * P_STAGE;
        lds_cptr SN = L + ((kc + 1) & 3) * P_STAGE;
        if (kc == TOUCH_KC && do_touch) {
#pragma unroll
            for (int i = 0; i < 8; ++i)
                asm volatile("global_load_dword %0, %1, off" : "+v"(touch) : "v"(dst + (i >> 2) * dm + ((i >> 1) & 1) * dc + (i & 1) * du) : "memory");
        }
#pragma unroll
        for (int s = 0; s < 4; ++s) {
            SBAR();
            if (s == 3 && kc < 7) LBAR();
            PMM(0, 0, s & 1);
            SBAR();
            if (s < 3) PRD(S, s + 1, (s + 1) & 1, 0); else PRD(SN, 0, 0, 0);
            SBAR();
            PMM(0, 1, s & 1);
            SBAR();
            if (s < 3) PRD(S, s + 1, (s + 1) & 1, 1); else PRD(SN, 0, 0, 1);
            SBAR();
            PMM(1, 0, s & 1);
            PMM(1, 1, s & 1);
        }
    }
#undef PMM
#undef PRD
}

__global__ __launch_bounds__(640) void k_proj(const u16* __restrict__ x, const u16* __restrict__ wq, const u16* __restrict__ wk,
                                                 const u16* __restrict__ wv, const u16* __restrict__ wqkv, const u16* __restrict__ woA,
                                                 const u16* __restrict__ woB, const u16* __restrict__ gw, char* __restrict__ ws) {
    extern __shared__ __attribute__((aligned(16))) char smem[];
    const int tid = threadIdx.x, lane = tid & 63, wid = __builtin_amdgcn_readfirstlane(tid >> 6);
    const int bid = blockIdx.x, grp = bid & 7, idx = bid >> 3;
    const int cj = idx >> 2;
    const int bi = (grp >> 1) * 4 + (idx & 3);
    const int bj = (cj >> 1) * 4 + (grp & 1) * 2 + (cj & 1);
    const unsigned lds0 = (unsigned)(uintptr_t)smem;
    lds_cptr L = (lds_cptr)smem;
    char* kvbase = ws + WS_KV;
    const u32x4 ones = {0x3f803f80u, 0x3f803f80u, 0x3f803f80u, 0x3f803f80u};
    STAMP_DECL; STAMP(0);
    if (wid >= 8) {
        const int erow = (bj >> 1) * 16 + (lane & 15), eg = lane >> 4;
        const unsigned ex0 = erow * 128 + 16 * (eg ^ ((erow >> 1) & 7)), ex1 = erow * 128 + 16 * ((4 + eg) ^ ((erow >> 1) & 7));
        lds_cptr ewp = L + P_EXTRA + (8 * eg + ((lane & 15) >> 2)) * 32 + 8 * (lane & 3);
        lds_cptr gwp = L + P_GW + eg * 16;
        f32x4 eacc = {};
        __builtin_amdgcn_s_setprio(3);
#pragma unroll 1
        for (int kc = 0; kc < 8; ++kc) {
            LBAR();
            lds_cptr S = L + (kc & 3) * P_STAGE;
            if (wid == 8) {
                eacc = MFMA16(ld128(S + ex0), cat8(ldtr(ewp + kc * 2048), ldtr(ewp + kc * 2048 + 128)), eacc);
                eacc = MFMA16(ld128(S + ex1), cat8(ldtr(ewp + kc * 2048 + 1024), ldtr(ewp + kc * 2048 + 1152)), eacc);
            } else {
                eacc = MFMA16(ld128(S + ex0), ld128(gwp + kc * 128), eacc);
                eacc = MFMA16(ld128(S + ex1), ld128(gwp + kc * 128 + 64), eacc);
            }
        }
        const int key0 = bi * 128 + (bj >> 1) * 16;
        if (wid == 8) {
            const unsigned e0 = cvtpk(eacc[0], eacc[1]), e1 = cvtpk(eacc[2], eacc[3]);
            char* tb = kvbase + (size_t)(key0 >> 5) * KV_TILE;
            const int u = (key0 >> 4) & 1, d = lane & 15;
            if (bj & 1) {
                *(u32x2*)(tb + 1024 + u * VBLK + (eg >> 1) * 256 + d * 16 + (eg & 1) * 8) = (u32x2){e0, e1};
                if (lane < 2) *(u32x4*)(tb + 1024 + u * VBLK + 512 + lane * 16) = lane ? (u32x4){0u, 0u, 0u, 0u} : ones;
            } else {
                u16* kb = (u16*)(tb + (d >> 3) * 512 + (u * 16 + 4 * eg) * 16 + (d & 7) * 2);
                kb[0] = (u16)e0; kb[8] = (u16)(e0 >> 16); kb[16] = (u16)e1; kb[24] = (u16)(e1 >> 16);
            }
        } else {
            if (!(bj & 1) && (lane & 15) == 0) {
                f32x4 g, og;
#pragma unroll
                for (int i = 0; i < 4; ++i) { g[i] = rbf(__builtin_amdgcn_rcpf(1.f + __expf(-rbf(eacc[i])))); og[i] = rbf(1.f - g[i]); }
                float* G = (float*)(ws + WS_G);
                *(f32x4*)(G + key0 + 4 * eg) = g; *(f32x4*)(G + SEQ + key0 + 4 * eg) = og;
            }
            if (bid == 0) *(unsigned*)(ws + WS_CNT + lane * 256) = 0u;
        }
        STAMP(6); STAMP_OUT(0);
        return;
    }
    if (wid >= 4) {
        const int lw = wid - 4;
        const u16* W; int ldw, col0;
        if (bj < 4) { W = wq; ldw = 512; col0 = bj * 128; } else { W = wqkv; ldw = 1536; col0 = (bj - 4) * 128; }
        const u16* xs; const u16* wsrc;
        {
            const int row = 8 * lw + (lane >> 3), ch = (lane & 7) ^ ((row >> 1) & 7);
            xs = x + (size_t)(bi * 128 + row) * DM + ch * 8;
            const int kr = 4 * lw + (lane >> 4), wch = (lane & 15) ^ wswz(kr);
            wsrc = W + (size_t)kr * ldw + col0 + wch * 8;
        }
#define PSTAGE(s) do { const unsigned base_ = lds0 + ((s) & (P_NST - 1)) * P_STAGE + lw * 1024; \
        _Pragma("unroll") for (int i = 0; i < 4; ++i) { glds16(xs + (s) * 64 + i * 32 * DM, base_ + i * 4096); glds16(wsrc + ((s) * 64 + i * 16) * ldw, base_ + 16384 + i * 4096); } } while (0)
        PSTAGE(0);
        {
            const u16* we = (bj & 1) ? wv : wk;
#pragma unroll
            for (int i = 0; i < 4; ++i) glds16(we + (lw + 4 * i) * 512 + lane * 8, lds0 + P_EXTRA + (lw + 4 * i) * 1024);
            glds16(gw + lane * 8, lds0 + P_GW);
        }
        PSTAGE(1);
        STAMP(4);
        WAITV_BAR(8);
        PSTAGE(2);
#pragma unroll 1
        for (int kc = 1; kc < 6; ++kc) {
            WAITV_BAR(8);
            PSTAGE(kc + 2);
        }
        {
            const int ks = bid >> 2, kr = 4 * lw + (lane >> 4);
            const u16* wo = (ks < 32) ? woA + (size_t)ks * 16 * DM : woB + (size_t)(ks - 32) * 16 * DM;
            glds16(wo + (size_t)kr * DM + (bid & 3) * 128 + ((lane & 15) ^ wswz(kr)) * 8, lds0 + P_WO + lw * 1024);
        }
        WAITV_BAR(9);
        WAITV_BAR(0);
        {
            const int h = lane >> 5, blk = (lane >> 4) & 1, q = (lane & 15) >> 2, p = lane & 3;
            const int kr0 = 8 * h + q, kr1 = kr0 + 4, ch = 4 * lw + 2 * blk + (p >> 1);
            lds_cptr wb = L + P_WO + 8 * (p & 1);
            *(bf16x8*)(ws + WS_WO + ((size_t)((bid >> 2) * 16 + (bid & 3) * 4 + lw) * 64 + lane) * 16) =
                cat8(ldtr(wb + 256 * kr0 + 16 * (ch ^ wswz(kr0))), ldtr(wb + 256 * kr1 + 16 * (ch ^ wswz(kr1))));
        }
        STAMP(5); STAMP_OUT(0);
        return;
    }
    const int wr = wid >> 1, wc = wid & 1, r = lane & 31, h = lane >> 5;
    f32x16 acc[2][2] = {};
    unsigned xoff[2][4], woff[2][2];
#pragma unroll
    for (int m = 0; m < 2; ++m)
#pragma unroll
        for (int s = 0; s < 4; ++s) { const int row = wr * 64 + m * 32 + r; xoff[m][s] = row * 128 + 16 * ((2 * s + h) ^ ((row >> 1) & 7)); }
    {
        const int blk = (lane >> 4) & 1, q = (lane & 15) >> 2, p = lane & 3;
#pragma unroll
        for (int t = 0; t < 2; ++t) {
            const int krow = 8 * h + 4 * t + q;
#pragma unroll
            for (int c = 0; c < 2; ++c)
                woff[c][t] = 16384 + 256 * krow + 16 * ((4 * (wc * 2 + c) + 2 * blk + (p >> 1)) ^ wswz(krow)) + 8 * (p & 1);
        }
    }
    char* dst; int dm, dc, du;
    {
        const int srow0 = bi * 128 + wr * 64, hd0 = (bj & 3) * 8 + wc * 4;
        if (bj < 8) {
            dst = ws + WS_Q + (size_t)(((bj >> 2) * 32 + hd0) * SEQ + srow0) * 32 + lane * 16;
            dm = 32 * 32; du = SEQ * 32; dc = 2 * du;
        } else if (bj < 12) {
            dst = kvbase + (size_t)(1 + hd0) * KV_HEAD + (srow0 >> 5) * KV_TILE + h * 512 + r * 16;
            dm = (int)KV_TILE; du = (int)KV_HEAD; dc = 2 * du;
        } else {
            dst = kvbase + (size_t)(1 + hd0 + (r >> 4)) * KV_HEAD + (srow0 >> 5) * KV_TILE + 1024 + h * 256 + (r & 15) * 16;
            dm = (int)KV_TILE; du = VBLK; dc = 2 * (int)KV_HEAD;
        }
    }
    unsigned touch = 0;
    const bool do_touch = (bj < 8) ? (lane & 7) == 0 : ((idx & 3) == 0 && (lane & 3) == 0);
    STAMP(1);
    if (bj >= 12) proj_loop<true>(acc, L, xoff, woff, do_touch, touch, dst, dm, dc, du);
    else proj_loop<false>(acc, L, xoff, woff, do_touch, touch, dst, dm, dc, du);
    STAMP(2);
    asm volatile("s_waitcnt vmcnt(0)" : "+v"(touch) :: "memory");
    const float sc = (bj < 8) ? QSCALE : 1.f;
#pragma unroll
    for (int m = 0; m < 2; ++m)
#pragma unroll
        for (int c = 0; c < 2; ++c) {
            const f32x16 a = acc[m][c];
            unsigned w[8];
#pragma unroll
            for (int i = 0; i < 8; ++i) w[i] = cvtpk(a[2 * i] * sc, a[2 * i + 1] * sc);
#pragma unroll
            for (int u = 0; u < 2; ++u) {
                auto s0 = __builtin_amdgcn_permlane32_swap(w[4 * u + 0], w[4 * u + 2], false, false);
                auto s1 = __builtin_amdgcn_permlane32_swap(w[4 * u + 1], w[4 * u + 3], false, false);
                ST16A(dst + m * dm + c * dc + u * du, ((u32x4){s0[0], s1[0], s0[1], s1[1]}));
            }
        }
    if (bj >= 12 && (r & 15) < 2) {
        char* vb = dst - h * 256 - (r & 15) * 16 + h * VBLK + 512 + (r & 1) * 16;
        const u32x4 cv = (r & 1) ? (u32x4){0u, 0u, 0u, 0u} : ones;
#pragma unroll
        for (int m = 0; m < 2; ++m)
#pragma unroll
            for (int c = 0; c < 2; ++c) *(u32x4*)(vb + m * dm + c * dc) = cv;
    }
    STAMP(3); STAMP_OUT(0);
}

constexpr int A_RED = (int)KV_HEAD;
constexpr int A_GATE = A_RED + 24576, A_SYNC = A_GATE + 2048, A_LDS = A_SYNC + 64;

#define PIN(x) asm volatile("" : "+v"(x))
#define SMF(acc, vf, p) do { acc = __builtin_amdgcn_smfmac_f32_32x32x32_bf16(vf, __builtin_bit_cast(bf16x16, p), acc, sidx, 0, 0); PIN(acc); } while (0)
#define EXPR(S, lo, hi) _Pragma("unroll") for (int e_ = lo; e_ < hi; ++e_) S[e_] = __builtin_amdgcn_exp2f(S[e_])
#define LO8(S) __builtin_shufflevector(S, S, 0, 1, 2, 3, 4, 5, 6, 7)
#define HI8(S) __builtin_shufflevector(S, S, 8, 9, 10, 11, 12, 13, 14, 15)

__device__ __forceinline__ float max16(const f32x16& s) {
    float a = fmaxf(fmaxf(s[0], s[1]), s[2]), b = fmaxf(fmaxf(s[3], s[4]), s[5]);
    a = fmaxf(fmaxf(a, s[6]), s[7]); b = fmaxf(fmaxf(b, s[8]), s[9]);
    a = fmaxf(fmaxf(a, s[10]), s[11]); b = fmaxf(fmaxf(b, s[12]), s[13]);
    a = fmaxf(fmaxf(a, s[14]), s[15]);
    return fmaxf(a, b);
}
__device__ __forceinline__ float xhalf_max(float m) {
    auto rr = __builtin_amdgcn_permlane32_swap(__float_as_uint(m), __float_as_uint(m), false, false);
    return fmaxf(__uint_as_float(rr[0]), __uint_as_float(rr[1]));
}

#define ATT_STEP(KN, V1C, V0N) do { \
    SBAR(); EXPR(SA, 0, 2);   SBAR(); SMF(OB, v1, HI8(SB)); v1 = ld128(V1C); \
    SBAR(); EXPR(SA, 2, 6);   SBAR(); SB = MFMA32(kf, qbf, CB); PIN(SB); kf = ld128(KN); \
    SBAR(); EXPR(SA, 6, 10);  SBAR(); SMF(OA, v0, LO8(SA)); \
    SBAR(); EXPR(SA, 10, 16); EXPR(SB, 0, 2); SBAR(); SMF(OA, v1, HI8(SA)); \
    SBAR(); EXPR(SB, 2, 6);   SBAR(); SA = MFMA32(kf, qa, CA); PIN(SA); \
    SBAR(); EXPR(SB, 6, 10);  SBAR(); SMF(OB, v0, LO8(SB)); v0 = ld128(V0N); \
    SBAR(); EXPR(SB, 10, 16); } while (0)

#define POLL_SLEEP 4
#define LATE_DELAY 8
#define PRIO_C 5
typedef __attribute__((address_space(1))) unsigned gu32;
typedef __attribute__((address_space(3))) unsigned lu32;
__device__ __forceinline__ unsigned lds_ld(lu32* p) { return __builtin_amdgcn_readfirstlane(__hip_atomic_load(p, __ATOMIC_RELAXED, __HIP_MEMORY_SCOPE_WORKGROUP)); }
__device__ __forceinline__ void lds_arrive_wait(lu32* w, unsigned target, int lane) {
    asm volatile("s_waitcnt lgkmcnt(0)" ::: "memory");
    if (lane == 0) __hip_atomic_fetch_add(w, 1u, __ATOMIC_RELAXED, __HIP_MEMORY_SCOPE_WORKGROUP);
#pragma unroll 1
    for (unsigned spins = 0; spins < (1u << 22); ++spins) { if (lds_ld(w) >= target) break; __builtin_amdgcn_s_sleep(1); }
    asm volatile("" ::: "memory");
}

__global__ __launch_bounds__(512, 2) void k_attn(char* __restrict__ ws, u16* __restrict__ out) {
    extern __shared__ __attribute__((aligned(16))) char smem[];
    const int tid = threadIdx.x, lane = tid & 63, wid = __builtin_amdgcn_readfirstlane(tid >> 6);
    const int bid = blockIdx.x, grp = bid & 7, idx = bid >> 3;
    const int head = (idx >> 4) * 32 + (grp & 1) * 16 + (idx & 15), qb = grp >> 1;
    const int kvh = head < 32 ? 0 : head - 31;
    const unsigned lds0 = (unsigned)(uintptr_t)smem;
    const int q0 = qb * 512 + wid * 64;
    STAMP_DECL; STAMP(0);
    const int r = lane & 31, h = lane >> 5;
    bf16x8 qa, qbf;
    {
        const char* qsrc = ws + WS_Q + (size_t)(head * SEQ + q0) * 32 + lane * 16;
        asm volatile("global_load_dwordx4 %0, %2, off\n\tglobal_load_dwordx4 %1, %2, off offset:1024"
                     : "=&v"(qa), "=&v"(qbf) : "v"(qsrc) : "memory");
    }
    const char* ksrc = ws + WS_KV + (size_t)kvh * KV_HEAD + lane * 16;
#define KVDMA(p) do { int p_ = qb * 33 + (p); p_ = p_ >= 132 ? p_ - 132 : p_; glds16(ksrc + p_ * 1024, lds0 + (p) * 1024); } while (0)
    KVDMA(wid);
    STAMP(14);
    const __amdgpu_buffer_rsrc_t rsO = __builtin_amdgcn_make_buffer_rsrc((void*)(ws + WS_O), 0, 64 * SEQ * DH * 2, 0x00020000);
    const unsigned ooff = (unsigned)(head * SEQ + q0) * 32 + lane * 16;
    const u32x4 poison = {~0u, ~0u, ~0u, ~0u};
    lds_cptr L = (lds_cptr)smem;
    lu32* sync = (lu32*)(smem + A_SYNC);
    if (tid < 16) sync[tid] = 0u;
    lds_cptr kp = L + lane * 16;
    lds_cptr vp = L + 1024 + (r < 16 ? h * 256 + r * 16 : ((r == 16 || r == 20) ? 512 : 528));
    int sidx = 0xDDDD; PIN(sidx);
    f32x16 OA, OB, CA, CB, SA, SB;
#pragma unroll
    for (int i = 0; i < 16; ++i) { OA[i] = 0.f; OB[i] = 0.f; SB[i] = 0.f; }
    asm volatile("" : "+v"(OA), "+v"(OB), "+v"(SB));
    asm volatile("s_waitcnt vmcnt(0) lgkmcnt(0)\n\ts_barrier" : "+v"(qa), "+v"(qbf) :: "memory");
#define LANDED(w) do { if (lane == 0) __hip_atomic_fetch_add(sync + 8 + (w), 1u, __ATOMIC_RELAXED, __HIP_MEMORY_SCOPE_WORKGROUP); } while (0)
#define AWAIT(w) do { SBAR(); _Pragma("unroll 1") for (unsigned sp_ = 0; sp_ < (1u << 22); ++sp_) { if (lds_ld(sync + 8 + (w)) >= 4u) break; __builtin_amdgcn_s_sleep(1); } \
                      asm volatile("" ::: "memory"); SBAR(); } while (0)
    if (wid >= 4) {
#pragma unroll 1
        for (int i = 0; i < 8; ++i) KVDMA(4 + wid + 4 * i);
        asm volatile("s_waitcnt vmcnt(4)" ::: "memory"); LANDED(0);
#pragma unroll 1
        for (int i = 8; i < 16; ++i) KVDMA(4 + wid + 4 * i);
        asm volatile("s_waitcnt vmcnt(8)" ::: "memory"); LANDED(1);
#pragma unroll 1
        for (int i = 16; i < 24; ++i) KVDMA(4 + wid + 4 * i);
        asm volatile("s_waitcnt vmcnt(8)" ::: "memory"); LANDED(2);
#pragma unroll 1
        for (int i = 24; i < 31; ++i) KVDMA(4 + wid + 4 * i);
        if (wid < 6) glds16(ws + WS_G + ((head < 32 ? 0 : SEQ) + qb * 512) * 4 + (wid - 4) * 1024 + lane * 16, lds0 + A_GATE + (wid - 4) * 1024);
        __builtin_amdgcn_raw_buffer_store_b128(poison, rsO, ooff - 8192, 0, 16); __builtin_amdgcn_raw_buffer_store_b128(poison, rsO, ooff - 7168, 0, 16);
        __builtin_amdgcn_raw_buffer_store_b128(poison, rsO, ooff, 0, 16); __builtin_amdgcn_raw_buffer_store_b128(poison, rsO, ooff + 1024, 0, 16);
        asm volatile("s_waitcnt vmcnt(0)" ::: "memory");
        unsigned prev = 0;
        if (lane == 0) prev = __hip_atomic_fetch_add(sync + 8 + 3, 1u, __ATOMIC_RELAXED, __HIP_MEMORY_SCOPE_WORKGROUP);
        if (__builtin_amdgcn_readfirstlane(prev) == 3u && lane < 8)
            __hip_atomic_fetch_add((gu32*)(ws + WS_CNT + (qb * 8 + lane) * 256), 1u, __ATOMIC_RELAXED, __HIP_MEMORY_SCOPE_AGENT);
        AWAIT(3);
        __builtin_amdgcn_s_setprio(1);
    }
    STAMP(1);
    float ma, mb;
    bf16x8 kf, v0, v1;
    {
        const f32x16 z = {};
        kf = ld128(kp);
        const bf16x8 k1 = ld128(kp + KV_TILE), k2 = ld128(kp + 2 * KV_TILE), k3 = ld128(kp + 3 * KV_TILE);
        v0 = ld128(vp); v1 = ld128(vp + VBLK);
        const f32x16 s0 = MFMA32(kf, qa, z), s1 = MFMA32(k1, qa, z), s2 = MFMA32(kf, qbf, z), s3 = MFMA32(k1, qbf, z);
        ma = fmaxf(max16(s0), max16(s1)); mb = fmaxf(max16(s2), max16(s3));
        const f32x16 s4 = MFMA32(k2, qa, z), s5 = MFMA32(k3, qa, z), s6 = MFMA32(k2, qbf, z), s7 = MFMA32(k3, qbf, z);
        ma = xhalf_max(fmaxf(ma, fmaxf(max16(s4), max16(s5))));
        mb = xhalf_max(fmaxf(mb, fmaxf(max16(s6), max16(s7))));
    }
    for (int attempt = 0; attempt < 2; ++attempt) {
#pragma unroll
        for (int i = 0; i < 16; ++i) { CA[i] = -ma; CB[i] = -mb; }
        asm volatile("" : "+v"(CA), "+v"(CB));
        SA = MFMA32(kf, qa, CA);
        if (attempt == 0) STAMP(2);
#pragma unroll 1
        for (int c = 0; c < 8; ++c) {
            if (c == PRIO_C) __builtin_amdgcn_s_setprio(0);
            if (c < 3 || c == 4) AWAIT(c < 3 ? c : 3);
            lds_cptr kc = kp + c * 8 * KV_TILE;
            lds_cptr vc = vp + c * 8 * KV_TILE;
#pragma unroll
            for (int t = 0; t < 8; ++t) {
                ATT_STEP(kc + (t + 1) * KV_TILE, vc + t * KV_TILE + VBLK, vc + (t + 1) * KV_TILE);
            }
        }
        SBAR();
        SMF(OB, v1, HI8(SB));
        if (attempt == 0) STAMP(3); else STAMP(5);
        const float la = OA[8], lb = OB[8];
        float oa = 0.f, ob = 0.f;
#pragma unroll
        for (int i = 0; i < 8; ++i) { oa += fabsf(OA[i]); ob += fabsf(OB[i]); }
        const bool ok = (la <= 0x1p125f) && (lb <= 0x1p125f) && (oa <= 0x1p126f) && (ob <= 0x1p126f);
        if (attempt == 1 || __all(ok)) break;
        const f32x16 z = {};
        float ta = -INFINITY, tb = -INFINITY;
#pragma unroll 1
        for (int t = 0; t < 64; ++t) {
            const bf16x8 k0 = ld128(kp + t * KV_TILE);
            ta = fmaxf(ta, max16(MFMA32(k0, qa, z)));
            tb = fmaxf(tb, max16(MFMA32(k0, qbf, z)));
        }
        ma = xhalf_max(ta); mb = xhalf_max(tb);
#pragma unroll
        for (int i = 0; i < 16; ++i) { OA[i] = 0.f; OB[i] = 0.f; SB[i] = 0.f; }
        kf = ld128(kp); v0 = ld128(vp); v1 = ld128(vp + VBLK);
    }
    {
        const float* gl = (const float*)(smem + A_GATE) + wid * 64 + r;
        const float sa = gl[0] * __builtin_amdgcn_rcpf(OA[8]), sb = gl[32] * __builtin_amdgcn_rcpf(OB[8]);
        unsigned w[4];
#pragma unroll
        for (int i = 0; i < 4; ++i) w[i] = min(cvtpk(OA[2 * i] * sa, OA[2 * i + 1] * sa), 0xfffffffeu);
        auto s0 = __builtin_amdgcn_permlane32_swap(w[0], w[2], false, false);
        auto s1 = __builtin_amdgcn_permlane32_swap(w[1], w[3], false, false);
        __builtin_amdgcn_raw_buffer_store_b128((u32x4){s0[0], s1[0], s0[1], s1[1]}, rsO, ooff, 0, 16);
#pragma unroll
        for (int i = 0; i < 4; ++i) w[i] = min(cvtpk(OB[2 * i] * sb, OB[2 * i + 1] * sb), 0xfffffffeu);
        auto t0 = __builtin_amdgcn_permlane32_swap(w[0], w[2], false, false);
        auto t1 = __builtin_amdgcn_permlane32_swap(w[1], w[3], false, false);
        __builtin_amdgcn_raw_buffer_store_b128((u32x4){t0[0], t1[0], t0[1], t1[1]}, rsO, ooff + 1024, 0, 16);
        if (wid >= 4) {
            if (lane == 0) __hip_atomic_fetch_add(sync + 1, 1u, __ATOMIC_RELAXED, __HIP_MEMORY_SCOPE_WORKGROUP);
            STAMP(4); STAMP_OUT(1);
            return;
        }
    }
    STAMP(4);
    const int bn = (grp & 1) * 4 + (idx >> 3), kq = wid;
    bf16x8 wf[16][2];
    {
        const int d0 = (kq + (r >> 4)) & 3, d1 = (kq + 2 + (r >> 4)) & 3;
        const char* Wp = ws + WS_WO + ((size_t)(kq * 16) * 16 + bn * 2) * 1024 + (h * 32 + (r & 15)) * 16;
        const char* Wp0 = Wp + (d0 >> 1) * 1024 + (d0 & 1) * 256;
        const char* Wp1 = Wp + (d1 >> 1) * 1024 + (d1 & 1) * 256;
#pragma unroll
        for (int k = 0; k < 16; ++k) { wf[k][0] = *(const bf16x8*)(Wp0 + k * 16384); wf[k][1] = *(const bf16x8*)(Wp1 + k * 16384); }
    }
    {
        gu32* cnt = (gu32*)(ws + WS_CNT + (qb * 8 + (idx & 7)) * 256);
#pragma unroll 1
        for (unsigned spins = 0; spins < (1u << 20); ++spins) {
            if ((unsigned)__builtin_amdgcn_readfirstlane(__hip_atomic_load(cnt, __ATOMIC_RELAXED, __HIP_MEMORY_SCOPE_AGENT)) >= 64u) break;
            __builtin_amdgcn_s_sleep(8);
        }
        asm volatile("" ::: "memory");
    }
    f32x4* red = (f32x4*)(smem + A_RED);
#pragma unroll 1
    for (int ph = 0; ph < 2; ++ph) {
        const int row0 = qb * 512 + ph * 256 + (idx & 7) * 32;
        const unsigned aoff = (unsigned)(kq * 16 * SEQ + row0) * 32 + lane * 16;
        if (ph == 1) {
#pragma unroll 1
            for (unsigned spins = 0; spins < (1u << 24); ++spins) { if (lds_ld(sync + 1) >= 4u) break; __builtin_amdgcn_s_sleep(4); }
            __builtin_amdgcn_s_sleep(LATE_DELAY);
        }
        if (ph == 0) STAMP(7); else STAMP(10);
        f32x16 acc[2];
#pragma unroll 1
        for (unsigned spins = 0; spins < (1u << 20); ++spins) {
            asm volatile("" ::: "memory");
            u32x4 of[16];
#pragma unroll
            for (int k = 0; k < 16; ++k) of[k] = __builtin_amdgcn_raw_buffer_load_b128(rsO, aoff, k * SEQ * 32, 16);
            SBAR();
            acc[0] = (f32x16){}; acc[1] = (f32x16){};
            unsigned mx = 0;
#pragma unroll
            for (int k = 0; k < 16; ++k) {
                acc[0] = MFMA32(wf[k][0], __builtin_bit_cast(bf16x8, of[k]), acc[0]);
                acc[1] = MFMA32(wf[k][1], __builtin_bit_cast(bf16x8, of[k]), acc[1]);
                mx = max(max(mx, max(of[k][0], of[k][1])), max(of[k][2], of[k][3]));
                PIN(mx); SBAR();
            }
            if (!__any(mx == ~0u)) break;
            __builtin_amdgcn_s_sleep(POLL_SLEEP);
        }
        if (ph == 0) STAMP(8); else STAMP(11);
        f32x4 own[2];
#pragma unroll
        for (int j = 0; j < 4; ++j)
#pragma unroll
            for (int g = 0; g < 2; ++g) {
                const int e = 4 * (2 * (j & 1) + g);
                const f32x4 v = {acc[j >> 1][e], acc[j >> 1][e + 1], acc[j >> 1][e + 2], acc[j >> 1][e + 3]};
                if (j == 0) own[g] = v;
                else red[((((kq + j) & 3) * 3 + j - 1) * 2 + g) * 64 + lane] = v;
            }
        if (ph == 0) lds_arrive_wait(sync + 4, 4u, lane);
        else LBAR();
#pragma unroll
        for (int sl = 0; sl < 3; ++sl)
#pragma unroll
            for (int g = 0; g < 2; ++g) own[g] += red[((kq * 3 + sl) * 2 + g) * 64 + lane];
        auto s0 = __builtin_amdgcn_permlane32_swap(cvtpk(own[0][0], own[0][1]), cvtpk(own[1][0], own[1][1]), false, false);
        auto s1 = __builtin_amdgcn_permlane32_swap(cvtpk(own[0][2], own[0][3]), cvtpk(own[1][2], own[1][3]), false, false);
        ST16B(out + (size_t)(row0 + r) * DM + bn * 64 + kq * 16 + h * 8, ((u32x4){s0[0], s1[0], s0[1], s1[1]}));
        if (ph == 0) { lds_arrive_wait(sync + 5, 4u, lane); STAMP(9); }
    }
    STAMP(12); STAMP_OUT(1);
}

extern "C" void kernel_launch(void* const* d_in, const int* in_sizes, int n_in,
                              void* d_out, int out_size, void* d_ws, size_t ws_size,
                              hipStream_t stream) {
    const u16* x = (const u16*)d_in[0];
    const u16* wq = (const u16*)d_in[1];
    const u16* wk = (const u16*)d_in[2];
    const u16* wv = (const u16*)d_in[3];
    const u16* woA = (const u16*)d_in[4];
    const u16* wqkv = (const u16*)d_in[6];
    const u16* woB = (const u16*)d_in[7];
    const u16* gw = (const u16*)d_in[8];
    char* ws = (char*)d_ws;
    (void)hipFuncSetAttribute((const void*)k_proj, hipFuncAttributeMaxDynamicSharedMemorySize, P_LDS);
    (void)hipFuncSetAttribute((const void*)k_attn, hipFuncAttributeMaxDynamicSharedMemorySize, A_LDS);
    k_proj<<<256, 640, P_LDS, stream>>>(x, wq, wk, wv, wqkv, woA, woB, gw, ws);
    k_attn<<<256, 512, A_LDS, stream>>>(ws, (u16*)d_out);
}
```

```cpp
#include <hip/hip_runtime.h>
#include <hip/hip_bf16.h>
#include <stdint.h>

typedef __attribute__((ext_vector_type(8))) __bf16 bf16x8;
typedef __attribute__((ext_vector_type(16))) __bf16 bf16x16;
typedef __attribute__((ext_vector_type(4))) short s16x4;
typedef __attribute__((ext_vector_type(8))) short s16x8;
typedef __attribute__((ext_vector_type(4))) float f32x4;
typedef __attribute__((ext_vector_type(8))) float f32x8;
typedef __attribute__((ext_vector_type(16))) float f32x16;
typedef __attribute__((ext_vector_type(4))) unsigned u32x4;
typedef __attribute__((ext_vector_type(2))) unsigned u32x2;
typedef unsigned short u16;
typedef __attribute__((address_space(3))) const char* lds_cptr;
typedef __attribute__((address_space(3))) char* lds_ptr;

constexpr int SEQ = 2048, DM = 512, NH = 32, DH = 16;
constexpr float QSCALE = 0.25f * 1.4426950408889634f;

constexpr int VBLK = 544;
constexpr size_t KV_TILE = 1024 + 2 * VBLK;
constexpr size_t KV_HEAD = 64 * KV_TILE;
constexpr size_t WS_Q = 0;
constexpr size_t WS_O = WS_Q + (size_t)64 * SEQ * DH * 2;
constexpr size_t WS_WO = WS_O + (size_t)64 * SEQ * DH * 2;
constexpr size_t WS_G = WS_WO + (size_t)1024 * 512 * 2;
constexpr size_t WS_CNT = WS_G + 2 * SEQ * 4;
constexpr size_t WS_KV = WS_CNT + 64 * 256;
constexpr size_t WS_END = WS_KV + 34 * KV_HEAD;

#define SBAR() __builtin_amdgcn_sched_barrier(0)
#define MFMA32(a, b, c) __builtin_amdgcn_mfma_f32_32x32x16_bf16(a, b, c, 0, 0, 0)
#define MFMA16(a, b, c) __builtin_amdgcn_mfma_f32_16x16x32_bf16(a, b, c, 0, 0, 0)

typedef __attribute__((ext_vector_type(2))) __bf16 bf16x2;
typedef __attribute__((ext_vector_type(2))) float f32x2;
__device__ __forceinline__ unsigned cvtpk(float lo, float hi) {
    return __builtin_bit_cast(unsigned, __builtin_convertvector((f32x2){lo, hi}, bf16x2));
}
#define STAMP(i)
#define STAMP_DECL
#define STAMP_OUT(k)
__device__ __forceinline__ float bf2f(u16 v) { return __uint_as_float((unsigned)v << 16); }
__device__ __forceinline__ float rbf(float v) { return __uint_as_float(cvtpk(v, 0.f) << 16); }

__device__ __forceinline__ void glds16(const void* g, unsigned lds_base) {
    unsigned sv;
    asm volatile("s_mov_b32 %0, m0\n\ts_mov_b32 m0, %2\n\ts_nop 0\n\tglobal_load_lds_dwordx4 %1, off\n\ts_mov_b32 m0, %0"
                 : "=&s"(sv) : "v"(g), "s"(lds_base) : "memory");
}
__device__ __forceinline__ s16x4 ldtr(lds_cptr p) {
    typedef short v4i16_t __attribute__((ext_vector_type(4)));
    return __builtin_bit_cast(s16x4, __builtin_amdgcn_ds_read_tr16_b64_v4i16((__attribute__((address_space(3))) v4i16_t*)p));
}
__device__ __forceinline__ bf16x8 ld128(lds_cptr p) { return *(const __attribute__((address_space(3))) bf16x8*)p; }
__device__ __forceinline__ bf16x8 cat8(s16x4 a, s16x4 b) {
    s16x8 v = {a[0], a[1], a[2], a[3], b[0], b[1], b[2], b[3]}; return __builtin_bit_cast(bf16x8, v);
}
#define ST16_PLAIN(p, v) (*(u32x4*)(p) = (v))
#define ST16_NT(p, v) __builtin_nontemporal_store((u32x4)(v), (u32x4*)(p))
#define ST16_SC1(p, v) do { u32x4 v_ = (v); asm volatile("global_store_dwordx4 %0, %1, off sc1\n\ts_nop 1" :: "v"(p), "v"(v_) : "memory"); } while (0)
#define ST16A ST16_NT
#define ST16B ST16_PLAIN
#define WAITV_BAR(N) asm volatile("s_waitcnt vmcnt(" #N ") lgkmcnt(0)\n\ts_barrier" ::: "memory")

constexpr int P_STAGE = 32768, P_NST = 4, P_EXTRA = P_NST * P_STAGE, P_GW = P_EXTRA + 16384, P_WO = P_GW + 1024, P_LDS = P_WO + 4096;

__device__ __forceinline__ unsigned wswz(unsigned row) { return ((row & 3) << 2) | ((row >> 2) & 3); }
#define LBAR() asm volatile("s_waitcnt lgkmcnt(0)\n\ts_barrier" ::: "memory")

#define PROJ_CODE_LINES 56
#define ATTN_CODE_LINES 64
#define TOUCH_KC 2
template <bool VT>
__device__ __forceinline__ void proj_loop(f32x16 (&acc)[2][2], lds_cptr L, const unsigned (&xoff)[2][4], const unsigned (&woff)[2][2],
                                          bool do_touch, unsigned& touch, const char* dst, int dm, int dc, int du) {
    bf16x8 xf[2][2], wf[2][2];
#define PMM(m, c, buf) acc[m][c] = VT ? MFMA32(xf[buf][m], wf[buf][c], acc[m][c]) : MFMA32(wf[buf][c], xf[buf][m], acc[m][c])
#define PRD(S, s, buf, i) do { xf[buf][i] = ld128(S + xoff[i][s]); wf[buf][i] = cat8(ldtr(S + woff[i][0] + (s) * 4096), ldtr(S + woff[i][1] + (s) * 4096)); } while (0)
    LBAR();
    PRD(L, 0, 0, 0); PRD(L, 0, 0, 1);
#pragma unroll 1
    for (int kc = 0; kc < 8; ++kc) {
        lds_cptr S = L + (kc & 3) * P_STAGE;
        lds_cptr SN = L + ((kc + 1) & 3) * P_STAGE;
        if (kc == TOUCH_KC && do_touch) {
#pragma unroll
            for (int i = 0; i < 8; ++i)
                asm volatile("global_load_dword %0, %1, off" : "+v"(touch) : "v"(dst + (i >> 2) * dm + ((i >> 1) & 1) * dc + (i & 1) * du) : "memory");
        }
#pragma unroll
        for (int s = 0; s < 4; ++s) {
            SBAR();
            if (s == 3 && kc < 7) LBAR();
            PMM(0, 0, s & 1);
            SBAR();
            if (s < 3) PRD(S, s + 1, (s + 1) & 1, 0); else PRD(SN, 0, 0, 0);
            SBAR();
            PMM(0, 1, s & 1);
            SBAR();
            if (s < 3) PRD(S, s + 1, (s + 1) & 1, 1); else PRD(SN, 0, 0, 1);
            SBAR();
            PMM(1, 0, s & 1);
            PMM(1, 1, s & 1);
        }
    }
#undef PMM
#undef PRD
}

__global__ __launch_bounds__(640) void k_proj(const u16* __restrict__ x, const u16* __restrict__ wq, const u16* __restrict__ wk,
                                                 const u16* __restrict__ wv, const u16* __restrict__ wqkv, const u16* __restrict__ woA,
                                                 const u16* __restrict__ woB, const u16* __restrict__ gw, char* __restrict__ ws) {
    extern __shared__ __attribute__((aligned(16))) char smem[];
    const int tid = threadIdx.x, lane = tid & 63, wid = __builtin_amdgcn_readfirstlane(tid >> 6);
    const int bid = blockIdx.x, grp = bid & 7, idx = bid >> 3;
    unsigned ctouch = 0;
    if (wid >= 8 && idx == 0) {
        unsigned long long pc; int rel0, rel1;
        asm volatile("s_getpc_b64 %0\n\ts_mov_b32 %1, _Z6k_projPKtS0_S0_S0_S0_S0_S0_S0_Pc@rel32@lo+4\n\ts_mov_b32 %2, _Z6k_attnPcPt@rel32@lo+12"
                     : "=s"(pc), "=s"(rel0), "=s"(rel1));
        pc += (long long)(wid == 8 ? rel0 : rel1);
        if (lane < (wid == 8 ? PROJ_CODE_LINES : ATTN_CODE_LINES))
            asm volatile("global_load_dword %0, %1, off" : "+v"(ctouch) : "v"((const char*)pc + lane * 128) : "memory");
    }
    const int cj = idx >> 2;
    const int bi = (grp >> 1) * 4 + (idx & 3);
    const int bj = (cj >> 1) * 4 + (grp & 1) * 2 + (cj & 1);
    const unsigned lds0 = (unsigned)(uintptr_t)smem;
    lds_cptr L = (lds_cptr)smem;
    char* kvbase = ws + WS_KV;
    const u32x4 ones = {0x3f803f80u, 0x3f803f80u, 0x3f803f80u, 0x3f803f80u};
    STAMP_DECL; STAMP(0);
    if (wid >= 8) {
        const int erow = (bj >> 1) * 16 + (lane & 15), eg = lane >> 4;
        const unsigned ex0 = erow * 128 + 16 * (eg ^ ((erow >> 1) & 7)), ex1 = erow * 128 + 16 * ((4 + eg) ^ ((erow >> 1) & 7));
        lds_cptr ewp = L + P_EXTRA + (8 * eg + ((lane & 15) >> 2)) * 32 + 8 * (lane & 3);
        lds_cptr gwp = L + P_GW + eg * 16;
        f32x4 eacc = {};
        __builtin_amdgcn_s_setprio(3);
#pragma unroll 1
        for (int kc = 0; kc < 8; ++kc) {
            LBAR();
            lds_cptr S = L + (kc & 3) * P_STAGE;
            if (wid == 8) {
                eacc = MFMA16(ld128(S + ex0), cat8(ldtr(ewp + kc * 2048), ldtr(ewp + kc * 2048 + 128)), eacc);
                eacc = MFMA16(ld128(S + ex1), cat8(ldtr(ewp + kc * 2048 + 1024), ldtr(ewp + kc * 2048 + 1152)), eacc);
            } else {
                eacc = MFMA16(ld128(S + ex0), ld128(gwp + kc * 128), eacc);
                eacc = MFMA16(ld128(S + ex1), ld128(gwp + kc * 128 + 64), eacc);
            }
        }
        asm volatile("s_waitcnt vmcnt(0)" : "+v"(ctouch) :: "memory");
        const int key0 = bi * 128 + (bj >> 1) * 16;
        if (wid == 8) {
            const unsigned e0 = cvtpk(eacc[0], eacc[1]), e1 = cvtpk(eacc[2], eacc[3]);
            char* tb = kvbase + (size_t)(key0 >> 5) * KV_TILE;
            const int u = (key0 >> 4) & 1, d = lane & 15;
            if (bj & 1) {
                *(u32x2*)(tb + 1024 + u * VBLK + (eg >> 1) * 256 + d * 16 + (eg & 1) * 8) = (u32x2){e0, e1};
                if (lane < 2) *(u32x4*)(tb + 1024 + u * VBLK + 512 + lane * 16) = lane ? (u32x4){0u, 0u, 0u, 0u} : ones;
            } else {
                u16* kb = (u16*)(tb + (d >> 3) * 512 + (u * 16 + 4 * eg) * 16 + (d & 7) * 2);
                kb[0] = (u16)e0; kb[8] = (u16)(e0 >> 16); kb[16] = (u16)e1; kb[24] = (u16)(e1 >> 16);
            }
        } else {
            if (!(bj & 1) && (lane & 15) == 0) {
                f32x4 g, og;
#pragma unroll
                for (int i = 0; i < 4; ++i) { g[i] = rbf(__builtin_amdgcn_rcpf(1.f + __expf(-rbf(eacc[i])))); og[i] = rbf(1.f - g[i]); }
                float* G = (float*)(ws + WS_G);
                *(f32x4*)(G + key0 + 4 * eg) = g; *(f32x4*)(G + SEQ + key0 + 4 * eg) = og;
            }
            if (bid == 0) *(unsigned*)(ws + WS_CNT + lane * 256) = 0u;
        }
        STAMP(6); STAMP_OUT(0);
        return;
    }
    if (wid >= 4) {
        const int lw = wid - 4;
        const u16* W; int ldw, col0;
        if (bj < 4) { W = wq; ldw = 512; col0 = bj * 128; } else { W = wqkv; ldw = 1536; col0 = (bj - 4) * 128; }
        const u16* xs; const u16* wsrc;
        {
            const int row = 8 * lw + (lane >> 3), ch = (lane & 7) ^ ((row >> 1) & 7);
            xs = x + (size_t)(bi * 128 + row) * DM + ch * 8;
            const int kr = 4 * lw + (lane >> 4), wch = (lane & 15) ^ wswz(kr);
            wsrc = W + (size_t)kr * ldw + col0 + wch * 8;
        }
#define PSTAGE(s) do { const unsigned base_ = lds0 + ((s) & (P_NST - 1)) * P_STAGE + lw * 1024; \
        _Pragma("unroll") for (int i = 0; i < 4; ++i) { glds16(xs + (s) * 64 + i * 32 * DM, base_ + i * 4096); glds16(wsrc + ((s) * 64 + i * 16) * ldw, base_ + 16384 + i * 4096); } } while (0)
        PSTAGE(0);
        {
            const u16* we = (bj & 1) ? wv : wk;
#pragma unroll
            for (int i = 0; i < 4; ++i) glds16(we + (lw + 4 * i) * 512 + lane * 8, lds0 + P_EXTRA + (lw + 4 * i) * 1024);
            glds16(gw + lane * 8, lds0 + P_GW);
        }
        PSTAGE(1);
        STAMP(4);
        WAITV_BAR(8);
        PSTAGE(2);
#pragma unroll 1
        for (int kc = 1; kc < 6; ++kc) {
            WAITV_BAR(8);
            PSTAGE(kc + 2);
        }
        {
            const int ks = bid >> 2, kr = 4 * lw + (lane >> 4);
            const u16* wo = (ks < 32) ? woA + (size_t)ks * 16 * DM : woB + (size_t)(ks - 32) * 16 * DM;
            glds16(wo + (size_t)kr * DM + (bid & 3) * 128 + ((lane & 15) ^ wswz(kr)) * 8, lds0 + P_WO + lw * 1024);
        }
        WAITV_BAR(9);
        WAITV_BAR(0);
        {
            const int h = lane >> 5, blk = (lane >> 4) & 1, q = (lane & 15) >> 2, p = lane & 3;
            const int kr0 = 8 * h + q, kr1 = kr0 + 4, ch = 4 * lw + 2 * blk + (p >> 1);
            lds_cptr wb = L + P_WO + 8 * (p & 1);
            *(bf16x8*)(ws + WS_WO + ((size_t)((bid >> 2) * 16 + (bid & 3) * 4 + lw) * 64 + lane) * 16) =
                cat8(ldtr(wb + 256 * kr0 + 16 * (ch ^ wswz(kr0))), ldtr(wb + 256 * kr1 + 16 * (ch ^ wswz(kr1))));
        }
        STAMP(5); STAMP_OUT(0);
        return;
    }
    const int wr = wid >> 1, wc = wid & 1, r = lane & 31, h = lane >> 5;
    f32x16 acc[2][2] = {};
    unsigned xoff[2][4], woff[2][2];
#pragma unroll
    for (int m = 0; m < 2; ++m)
#pragma unroll
        for (int s = 0; s < 4; ++s) { const int row = wr * 64 + m * 32 + r; xoff[m][s] = row * 128 + 16 * ((2 * s + h) ^ ((row >> 1) & 7)); }
    {
        const int blk = (lane >> 4) & 1, q = (lane & 15) >> 2, p = lane & 3;
#pragma unroll
        for (int t = 0; t < 2; ++t) {
            const int krow = 8 * h + 4 * t + q;
#pragma unroll
            for (int c = 0; c < 2; ++c)
                woff[c][t] = 16384 + 256 * krow + 16 * ((4 * (wc * 2 + c) + 2 * blk + (p >> 1)) ^ wswz(krow)) + 8 * (p & 1);
        }
    }
    char* dst; int dm, dc, du;
    {
        const int srow0 = bi * 128 + wr * 64, hd0 = (bj & 3) * 8 + wc * 4;
        if (bj < 8) {
            dst = ws + WS_Q + (size_t)(((bj >> 2) * 32 + hd0) * SEQ + srow0) * 32 + lane * 16;
            dm = 32 * 32; du = SEQ * 32; dc = 2 * du;
        } else if (bj < 12) {
            dst = kvbase + (size_t)(1 + hd0) * KV_HEAD + (srow0 >> 5) * KV_TILE + h * 512 + r * 16;
            dm = (int)KV_TILE; du = (int)KV_HEAD; dc = 2 * du;
        } else {
            dst = kvbase + (size_t)(1 + hd0 + (r >> 4)) * KV_HEAD + (srow0 >> 5) * KV_TILE + 1024 + h * 256 + (r & 15) * 16;
            dm = (int)KV_TILE; du = VBLK; dc = 2 * (int)KV_HEAD;
        }
    }
    unsigned touch = 0;
    const bool do_touch = (bj < 8) ? (lane & 7) == 0 : ((idx & 3) == 0 && (lane & 3) == 0);
    STAMP(1);
    if (bj >= 12) proj_loop<true>(acc, L, xoff, woff, do_touch, touch, dst, dm, dc, du);
    else proj_loop<false>(acc, L, xoff, woff, do_touch, touch, dst, dm, dc, du);
    STAMP(2);
    asm volatile("s_waitcnt vmcnt(0)" : "+v"(touch) :: "memory");
    const float sc = (bj < 8) ? QSCALE : 1.f;
#pragma unroll
    for (int m = 0; m < 2; ++m)
#pragma unroll
        for (int c = 0; c < 2; ++c) {
            const f32x16 a = acc[m][c];
            unsigned w[8];
#pragma unroll
            for (int i = 0; i < 8; ++i) w[i] = cvtpk(a[2 * i] * sc, a[2 * i + 1] * sc);
#pragma unroll
            for (int u = 0; u < 2; ++u) {
                auto s0 = __builtin_amdgcn_permlane32_swap(w[4 * u + 0], w[4 * u + 2], false, false);
                auto s1 = __builtin_amdgcn_permlane32_swap(w[4 * u + 1], w[4 * u + 3], false, false);
                ST16A(dst + m * dm + c * dc + u * du, ((u32x4){s0[0], s1[0], s0[1], s1[1]}));
            }
        }
    if (bj >= 12 && (r & 15) < 2) {
        char* vb = dst - h * 256 - (r & 15) * 16 + h * VBLK + 512 + (r & 1) * 16;
        const u32x4 cv = (r & 1) ? (u32x4){0u, 0u, 0u, 0u} : ones;
#pragma unroll
        for (int m = 0; m < 2; ++m)
#pragma unroll
            for (int c = 0; c < 2; ++c) *(u32x4*)(vb + m * dm + c * dc) = cv;
    }
    STAMP(3); STAMP_OUT(0);
}

constexpr int A_RED = (int)KV_HEAD;
constexpr int A_GATE = A_RED + 24576, A_SYNC = A_GATE + 2048, A_LDS = A_SYNC + 64;

#define PIN(x) asm volatile("" : "+v"(x))
#define SMF(acc, vf, p) do { acc = __builtin_amdgcn_smfmac_f32_32x32x32_bf16(vf, __builtin_bit_cast(bf16x16, p), acc, sidx, 0, 0); PIN(acc); } while (0)
#define EXPR(S, lo, hi) _Pragma("unroll") for (int e_ = lo; e_ < hi; ++e_) S[e_] = __builtin_amdgcn_exp2f(S[e_])
#define LO8(S) __builtin_shufflevector(S, S, 0, 1, 2, 3, 4, 5, 6, 7)
#define HI8(S) __builtin_shufflevector(S, S, 8, 9, 10, 11, 12, 13, 14, 15)

__device__ __forceinline__ float max16(const f32x16& s) {
    float a = fmaxf(fmaxf(s[0], s[1]), s[2]), b = fmaxf(fmaxf(s[3], s[4]), s[5]);
    a = fmaxf(fmaxf(a, s[6]), s[7]); b = fmaxf(fmaxf(b, s[8]), s[9]);
    a = fmaxf(fmaxf(a, s[10]), s[11]); b = fmaxf(fmaxf(b, s[12]), s[13]);
    a = fmaxf(fmaxf(a, s[14]), s[15]);
    return fmaxf(a, b);
}
__device__ __forceinline__ float xhalf_max(float m) {
    auto rr = __builtin_amdgcn_permlane32_swap(__float_as_uint(m), __float_as_uint(m), false, false);
    return fmaxf(__uint_as_float(rr[0]), __uint_as_float(rr[1]));
}

#define ATT_STEP(KN, V1C, V0N) do { \
    SBAR(); EXPR(SA, 0, 2);   SBAR(); SMF(OB, v1, HI8(SB)); v1 = ld128(V1C); \
    SBAR(); EXPR(SA, 2, 6);   SBAR(); SB = MFMA32(kf, qbf, CB); PIN(SB); kf = ld128(KN); \
    SBAR(); EXPR(SA, 6, 10);  SBAR(); SMF(OA, v0, LO8(SA)); \
    SBAR(); EXPR(SA, 10, 16); EXPR(SB, 0, 2); SBAR(); SMF(OA, v1, HI8(SA)); \
    SBAR(); EXPR(SB, 2, 6);   SBAR(); SA = MFMA32(kf, qa, CA); PIN(SA); \
    SBAR(); EXPR(SB, 6, 10);  SBAR(); SMF(OB, v0, LO8(SB)); v0 = ld128(V0N); \
    SBAR(); EXPR(SB, 10, 16); } while (0)

#define POLL_SLEEP 4
#define LATE_DELAY 8
#define PRIO_C 5
typedef __attribute__((address_space(1))) unsigned gu32;
typedef __attribute__((address_space(3))) unsigned lu32;
__device__ __forceinline__ unsigned lds_ld(lu32* p) { return __builtin_amdgcn_readfirstlane(__hip_atomic_load(p, __ATOMIC_RELAXED, __HIP_MEMORY_SCOPE_WORKGROUP)); }
__device__ __forceinline__ void lds_arrive_wait(lu32* w, unsigned target, int lane) {
    asm volatile("s_waitcnt lgkmcnt(0)" ::: "memory");
    if (lane == 0) __hip_atomic_fetch_add(w, 1u, __ATOMIC_RELAXED, __HIP_MEMORY_SCOPE_WORKGROUP);
#pragma unroll 1
    for (unsigned spins = 0; spins < (1u << 22); ++spins) { if (lds_ld(w) >= target) break; __builtin_amdgcn_s_sleep(1); }
    asm volatile("" ::: "memory");
}

__global__ __launch_bounds__(512, 2) void k_attn(char* __restrict__ ws, u16* __restrict__ out) {
    extern __shared__ __attribute__((aligned(16))) char smem[];
    const int tid = threadIdx.x, lane = tid & 63, wid = __builtin_amdgcn_readfirstlane(tid >> 6);
    const int bid = blockIdx.x, grp = bid & 7, idx = bid >> 3;
    const int head = (idx >> 4) * 32 + (grp & 1) * 16 + (idx & 15), qb = grp >> 1;
    const int kvh = head < 32 ? 0 : head - 31;
    const unsigned lds0 = (unsigned)(uintptr_t)smem;
    const int q0 = qb * 512 + wid * 64;
    STAMP_DECL; STAMP(0);
    const int r = lane & 31, h = lane >> 5;
    bf16x8 qa, qbf;
    {
        const char* qsrc = ws + WS_Q + (size_t)(head * SEQ + q0) * 32 + lane * 16;
        asm volatile("global_load_dwordx4 %0, %2, off\n\tglobal_load_dwordx4 %1, %2, off offset:1024"
                     : "=&v"(qa), "=&v"(qbf) : "v"(qsrc) : "memory");
    }
    const char* ksrc = ws + WS_KV + (size_t)kvh * KV_HEAD + lane * 16;
#define KVDMA(p) do { int p_ = qb * 33 + (p); p_ = p_ >= 132 ? p_ - 132 : p_; glds16(ksrc + p_ * 1024, lds0 + (p) * 1024); } while (0)
    KVDMA(wid);
    STAMP(14);
    const __amdgpu_buffer_rsrc_t rsO = __builtin_amdgcn_make_buffer_rsrc((void*)(ws + WS_O), 0, 64 * SEQ * DH * 2, 0x00020000);
    const unsigned ooff = (unsigned)(head * SEQ + q0) * 32 + lane * 16;
    const u32x4 poison = {~0u, ~0u, ~0u, ~0u};
    lds_cptr L = (lds_cptr)smem;
    lu32* sync = (lu32*)(smem + A_SYNC);
    if (tid < 16) sync[tid] = 0u;
    lds_cptr kp = L + lane * 16;
    lds_cptr vp = L + 1024 + (r < 16 ? h * 256 + r * 16 : ((r == 16 || r == 20) ? 512 : 528));
    int sidx = 0xDDDD; PIN(sidx);
    f32x16 OA, OB, CA, CB, SA, SB;
#pragma unroll
    for (int i = 0; i < 16; ++i) { OA[i] = 0.f; OB[i] = 0.f; SB[i] = 0.f; }
    asm volatile("" : "+v"(OA), "+v"(OB), "+v"(SB));
    asm volatile("s_waitcnt vmcnt(0) lgkmcnt(0)\n\ts_barrier" : "+v"(qa), "+v"(qbf) :: "memory");
#define LANDED(w) do { if (lane == 0) __hip_atomic_fetch_add(sync + 8 + (w), 1u, __ATOMIC_RELAXED, __HIP_MEMORY_SCOPE_WORKGROUP); } while (0)
#define AWAIT(w) do { SBAR(); _Pragma("unroll 1") for (unsigned sp_ = 0; sp_ < (1u << 22); ++sp_) { if (lds_ld(sync + 8 + (w)) >= 4u) break; __builtin_amdgcn_s_sleep(1); } \
                      asm volatile("" ::: "memory"); SBAR(); } while (0)
    if (wid >= 4) {
#pragma unroll 1
        for (int i = 0; i < 8; ++i) KVDMA(4 + wid + 4 * i);
        asm volatile("s_waitcnt vmcnt(4)" ::: "memory"); LANDED(0);
#pragma unroll 1
        for (int i = 8; i < 16; ++i) KVDMA(4 + wid + 4 * i);
        asm volatile("s_waitcnt vmcnt(8)" ::: "memory"); LANDED(1);
#pragma unroll 1
        for (int i = 16; i < 24; ++i) KVDMA(4 + wid + 4 * i);
        asm volatile("s_waitcnt vmcnt(8)" ::: "memory"); LANDED(2);
#pragma unroll 1
        for (int i = 24; i < 31; ++i) KVDMA(4 + wid + 4 * i);
        if (wid < 6) glds16(ws + WS_G + ((head < 32 ? 0 : SEQ) + qb * 512) * 4 + (wid - 4) * 1024 + lane * 16, lds0 + A_GATE + (wid - 4) * 1024);
        __builtin_amdgcn_raw_buffer_store_b128(poison, rsO, ooff - 8192, 0, 16); __builtin_amdgcn_raw_buffer_store_b128(poison, rsO, ooff - 7168, 0, 16);
        __builtin_amdgcn_raw_buffer_store_b128(poison, rsO, ooff, 0, 16); __builtin_amdgcn_raw_buffer_store_b128(poison, rsO, ooff + 1024, 0, 16);
        asm volatile("s_waitcnt vmcnt(0)" ::: "memory");
        unsigned prev = 0;
        if (lane == 0) prev = __hip_atomic_fetch_add(sync + 8 + 3, 1u, __ATOMIC_RELAXED, __HIP_MEMORY_SCOPE_WORKGROUP);
        if (__builtin_amdgcn_readfirstlane(prev) == 3u && lane < 8)
            __hip_atomic_fetch_add((gu32*)(ws + WS_CNT + (qb * 8 + lane) * 256), 1u, __ATOMIC_RELAXED, __HIP_MEMORY_SCOPE_AGENT);
        AWAIT(3);
        __builtin_amdgcn_s_setprio(1);
    }
    STAMP(1);
    float ma, mb;
    bf16x8 kf, v0, v1;
    {
        const f32x16 z = {};
        kf = ld128(kp);
        const bf16x8 k1 = ld128(kp + KV_TILE), k2 = ld128(kp + 2 * KV_TILE), k3 = ld128(kp + 3 * KV_TILE);
        v0 = ld128(vp); v1 = ld128(vp + VBLK);
        const f32x16 s0 = MFMA32(kf, qa, z), s1 = MFMA32(k1, qa, z), s2 = MFMA32(kf, qbf, z), s3 = MFMA32(k1, qbf, z);
        ma = fmaxf(max16(s0), max16(s1)); mb = fmaxf(max16(s2), max16(s3));
        const f32x16 s4 = MFMA32(k2, qa, z), s5 = MFMA32(k3, qa, z), s6 = MFMA32(k2, qbf, z), s7 = MFMA32(k3, qbf, z);
        ma = xhalf_max(fmaxf(ma, fmaxf(max16(s4), max16(s5))));
        mb = xhalf_max(fmaxf(mb, fmaxf(max16(s6), max16(s7))));
    }
    for (int attempt = 0; attempt < 2; ++attempt) {
#pragma unroll
        for (int i = 0; i < 16; ++i) { CA[i] = -ma; CB[i] = -mb; }
        asm volatile("" : "+v"(CA), "+v"(CB));
        SA = MFMA32(kf, qa, CA);
        if (attempt == 0) STAMP(2);
#pragma unroll 1
        for (int c = 0; c < 8; ++c) {
            if (c == PRIO_C) __builtin_amdgcn_s_setprio(0);
            if (c < 3 || c == 4) AWAIT(c < 3 ? c : 3);
            lds_cptr kc = kp + c * 8 * KV_TILE;
            lds_cptr vc = vp + c * 8 * KV_TILE;
#pragma unroll
            for (int t = 0; t < 8; ++t) {
                ATT_STEP(kc + (t + 1) * KV_TILE, vc + t * KV_TILE + VBLK, vc + (t + 1) * KV_TILE);
            }
        }
        SBAR();
        SMF(OB, v1, HI8(SB));
        if (attempt == 0) STAMP(3); else STAMP(5);
        const float la = OA[8], lb = OB[8];
        float oa = 0.f, ob = 0.f;
#pragma unroll
        for (int i = 0; i < 8; ++i) { oa += fabsf(OA[i]); ob += fabsf(OB[i]); }
        const bool ok = (la <= 0x1p125f) && (lb <= 0x1p125f) && (oa <= 0x1p126f) && (ob <= 0x1p126f);
        if (attempt == 1 || __all(ok)) break;
        const f32x16 z = {};
        float ta = -INFINITY, tb = -INFINITY;
#pragma unroll 1
        for (int t = 0; t < 64; ++t) {
            const bf16x8 k0 = ld128(kp + t * KV_TILE);
            ta = fmaxf(ta, max16(MFMA32(k0, qa, z)));
            tb = fmaxf(tb, max16(MFMA32(k0, qbf, z)));
        }
        ma = xhalf_max(ta); mb = xhalf_max(tb);
#pragma unroll
        for (int i = 0; i < 16; ++i) { OA[i] = 0.f; OB[i] = 0.f; SB[i] = 0.f; }
        kf = ld128(kp); v0 = ld128(vp); v1 = ld128(vp + VBLK);
    }
    {
        const float* gl = (const float*)(smem + A_GATE) + wid * 64 + r;
        const float sa = gl[0] * __builtin_amdgcn_rcpf(OA[8]), sb = gl[32] * __builtin_amdgcn_rcpf(OB[8]);
        unsigned w[4];
#pragma unroll
        for (int i = 0; i < 4; ++i) w[i] = min(cvtpk(OA[2 * i] * sa, OA[2 * i + 1] * sa), 0xfffffffeu);
        auto s0 = __builtin_amdgcn_permlane32_swap(w[0], w[2], false, false);
        auto s1 = __builtin_amdgcn_permlane32_swap(w[1], w[3], false, false);
        __builtin_amdgcn_raw_buffer_store_b128((u32x4){s0[0], s1[0], s0[1], s1[1]}, rsO, ooff, 0, 16);
#pragma unroll
        for (int i = 0; i < 4; ++i) w[i] = min(cvtpk(OB[2 * i] * sb, OB[2 * i + 1] * sb), 0xfffffffeu);
        auto t0 = __builtin_amdgcn_permlane32_swap(w[0], w[2], false, false);
        auto t1 = __builtin_amdgcn_permlane32_swap(w[1], w[3], false, false);
        __builtin_amdgcn_raw_buffer_store_b128((u32x4){t0[0], t1[0], t0[1], t1[1]}, rsO, ooff + 1024, 0, 16);
        if (wid >= 4) {
            if (lane == 0) __hip_atomic_fetch_add(sync + 1, 1u, __ATOMIC_RELAXED, __HIP_MEMORY_SCOPE_WORKGROUP);
            STAMP(4); STAMP_OUT(1);
            return;
        }
    }
    STAMP(4);
    const int bn = (grp & 1) * 4 + (idx >> 3), kq = wid;
    bf16x8 wf[16][2];
    {
        const int d0 = (kq + (r >> 4)) & 3, d1 = (kq + 2 + (r >> 4)) & 3;
        const char* Wp = ws + WS_WO + ((size_t)(kq * 16) * 16 + bn * 2) * 1024 + (h * 32 + (r & 15)) * 16;
        const char* Wp0 = Wp + (d0 >> 1) * 1024 + (d0 & 1) * 256;
        const char* Wp1 = Wp + (d1 >> 1) * 1024 + (d1 & 1) * 256;
#pragma unroll
        for (int k = 0; k < 16; ++k) { wf[k][0] = *(const bf16x8*)(Wp0 + k * 16384); wf[k][1] = *(const bf16x8*)(Wp1 + k * 16384); }
    }
    {
        gu32* cnt = (gu32*)(ws + WS_CNT + (qb * 8 + (idx & 7)) * 256);
#pragma unroll 1
        for (unsigned spins = 0; spins < (1u << 20); ++spins) {
            if ((unsigned)__builtin_amdgcn_readfirstlane(__hip_atomic_load(cnt, __ATOMIC_RELAXED, __HIP_MEMORY_SCOPE_AGENT)) >= 64u) break;
            __builtin_amdgcn_s_sleep(8);
        }
        asm volatile("" ::: "memory");
    }
    f32x4* red = (f32x4*)(smem + A_RED);
#pragma unroll 1
    for (int ph = 0; ph < 2; ++ph) {
        const int row0 = qb * 512 + ph * 256 + (idx & 7) * 32;
        const unsigned aoff = (unsigned)(kq * 16 * SEQ + row0) * 32 + lane * 16;
        if (ph == 1) {
#pragma unroll 1
            for (unsigned spins = 0; spins < (1u << 24); ++spins) { if (lds_ld(sync + 1) >= 4u) break; __builtin_amdgcn_s_sleep(4); }
            __builtin_amdgcn_s_sleep(LATE_DELAY);
        }
        if (ph == 0) STAMP(7); else STAMP(10);
        f32x16 acc[2];
#pragma unroll 1
        for (unsigned spins = 0; spins < (1u << 20); ++spins) {
            asm volatile("" ::: "memory");
            u32x4 of[16];
#pragma unroll
            for (int k = 0; k < 16; ++k) of[k] = __builtin_amdgcn_raw_buffer_load_b128(rsO, aoff, k * SEQ * 32, 16);
            SBAR();
            acc[0] = (f32x16){}; acc[1] = (f32x16){};
            unsigned mx = 0;
#pragma unroll
            for (int k = 0; k < 16; ++k) {
                acc[0] = MFMA32(wf[k][0], __builtin_bit_cast(bf16x8, of[k]), acc[0]);
                acc[1] = MFMA32(wf[k][1], __builtin_bit_cast(bf16x8, of[k]), acc[1]);
                mx = max(max(mx, max(of[k][0], of[k][1])), max(of[k][2], of[k][3]));
                PIN(mx); SBAR();
            }
            if (!__any(mx == ~0u)) break;
            __builtin_amdgcn_s_sleep(POLL_SLEEP);
        }
        if (ph == 0) STAMP(8); else STAMP(11);
        f32x4 own[2];
#pragma unroll
        for (int j = 0; j < 4; ++j)
#pragma unroll
            for (int g = 0; g < 2; ++g) {
                const int e = 4 * (2 * (j & 1) + g);
                const f32x4 v = {acc[j >> 1][e], acc[j >> 1][e + 1], acc[j >> 1][e + 2], acc[j >> 1][e + 3]};
                if (j == 0) own[g] = v;
                else red[((((kq + j) & 3) * 3 + j - 1) * 2 + g) * 64 + lane] = v;
            }
        if (ph == 0) lds_arrive_wait(sync + 4, 4u, lane);
        else LBAR();
#pragma unroll
        for (int sl = 0; sl < 3; ++sl)
#pragma unroll
            for (int g = 0; g < 2; ++g) own[g] += red[((kq * 3 + sl) * 2 + g) * 64 + lane];
        auto s0 = __builtin_amdgcn_permlane32_swap(cvtpk(own[0][0], own[0][1]), cvtpk(own[1][0], own[1][1]), false, false);
        auto s1 = __builtin_amdgcn_permlane32_swap(cvtpk(own[0][2], own[0][3]), cvtpk(own[1][2], own[1][3]), false, false);
        ST16B(out + (size_t)(row0 + r) * DM + bn * 64 + kq * 16 + h * 8, ((u32x4){s0[0], s1[0], s0[1], s1[1]}));
        if (ph == 0) { lds_arrive_wait(sync + 5, 4u, lane); STAMP(9); }
    }
    STAMP(12); STAMP_OUT(1);
}

extern "C" void kernel_launch(void* const* d_in, const int* in_sizes, int n_in,
                              void* d_out, int out_size, void* d_ws, size_t ws_size,
                              hipStream_t stream) {
    const u16* x = (const u16*)d_in[0];
    const u16* wq = (const u16*)d_in[1];
    const u16* wk = (const u16*)d_in[2];
    const u16* wv = (const u16*)d_in[3];
    const u16* woA = (const u16*)d_in[4];
    const u16* wqkv = (const u16*)d_in[6];
    const u16* woB = (const u16*)d_in[7];
    const u16* gw = (const u16*)d_in[8];
    char* ws = (char*)d_ws;
    (void)hipFuncSetAttribute((const void*)k_proj, hipFuncAttributeMaxDynamicSharedMemorySize, P_LDS);
    (void)hipFuncSetAttribute((const void*)k_attn, hipFuncAttributeMaxDynamicSharedMemorySize, A_LDS);
    k_proj<<<256, 640, P_LDS, stream>>>(x, wq, wk, wv, wqkv, woA, woB, gw, ws);
    k_attn<<<256, 512, A_LDS, stream>>>(ws, (u16*)d_out);
}
```

```cpp
#include <hip/hip_runtime.h>
#include <hip/hip_bf16.h>
#include <stdint.h>

typedef __attribute__((ext_vector_type(8))) __bf16 bf16x8;
typedef __attribute__((ext_vector_type(16))) __bf16 bf16x16;
typedef __attribute__((ext_vector_type(4))) short s16x4;
typedef __attribute__((ext_vector_type(8))) short s16x8;
typedef __attribute__((ext_vector_type(4))) float f32x4;
typedef __attribute__((ext_vector_type(8))) float f32x8;
typedef __attribute__((ext_vector_type(16))) float f32x16;
typedef __attribute__((ext_vector_type(4))) unsigned u32x4;
typedef __attribute__((ext_vector_type(2))) unsigned u32x2;
typedef unsigned short u16;
typedef __attribute__((address_space(3))) const char* lds_cptr;
typedef __attribute__((address_space(3))) char* lds_ptr;

constexpr int SEQ = 2048, DM = 512, NH = 32, DH = 16;
constexpr float QSCALE = 0.25f * 1.4426950408889634f;

constexpr int VBLK = 544;
constexpr size_t KV_TILE = 1024 + 2 * VBLK;
constexpr size_t KV_HEAD = 64 * KV_TILE;
constexpr size_t WS_Q = 0;
constexpr size_t WS_O = WS_Q + (size_t)64 * SEQ * DH * 2;
constexpr size_t WS_WO = WS_O + (size_t)64 * SEQ * DH * 2;
constexpr size_t WS_G = WS_WO + (size_t)1024 * 512 * 2;
constexpr size_t WS_CNT = WS_G + 2 * SEQ * 4;
constexpr size_t WS_KV = WS_CNT + 64 * 256;
constexpr size_t WS_END = WS_KV + 34 * KV_HEAD;

#define SBAR() __builtin_amdgcn_sched_barrier(0)
#define MFMA32(a, b, c) __builtin_amdgcn_mfma_f32_32x32x16_bf16(a, b, c, 0, 0, 0)
#define MFMA16(a, b, c) __builtin_amdgcn_mfma_f32_16x16x32_bf16(a, b, c, 0, 0, 0)

typedef __attribute__((ext_vector_type(2))) __bf16 bf16x2;
typedef __attribute__((ext_vector_type(2))) float f32x2;
__device__ __forceinline__ unsigned cvtpk(float lo, float hi) {
    return __builtin_bit_cast(unsigned, __builtin_convertvector((f32x2){lo, hi}, bf16x2));
}
#define STAMP(i)
#define STAMP_DECL
#define STAMP_OUT(k)
__device__ __forceinline__ float bf2f(u16 v) { return __uint_as_float((unsigned)v << 16); }
__device__ __forceinline__ float rbf(float v) { return __uint_as_float(cvtpk(v, 0.f) << 16); }

__device__ __forceinline__ void glds16(const void* g, unsigned lds_base) {
    unsigned sv;
    asm volatile("s_mov_b32 %0, m0\n\ts_mov_b32 m0, %2\n\ts_nop 0\n\tglobal_load_lds_dwordx4 %1, off\n\ts_mov_b32 m0, %0"
                 : "=&s"(sv) : "v"(g), "s"(lds_base) : "memory");
}
__device__ __forceinline__ s16x4 ldtr(lds_cptr p) {
    typedef short v4i16_t __attribute__((ext_vector_type(4)));
    return __builtin_bit_cast(s16x4, __builtin_amdgcn_ds_read_tr16_b64_v4i16((__attribute__((address_space(3))) v4i16_t*)p));
}
__device__ __forceinline__ bf16x8 ld128(lds_cptr p) { return *(const __attribute__((address_space(3))) bf16x8*)p; }
__device__ __forceinline__ bf16x8 cat8(s16x4 a, s16x4 b) {
    s16x8 v = {a[0], a[1], a[2], a[3], b[0], b[1], b[2], b[3]}; return __builtin_bit_cast(bf16x8, v);
}
#define ST16_PLAIN(p, v) (*(u32x4*)(p) = (v))
#define ST16_NT(p, v) __builtin_nontemporal_store((u32x4)(v), (u32x4*)(p))
#define ST16_SC1(p, v) do { u32x4 v_ = (v); asm volatile("global_store_dwordx4 %0, %1, off sc1\n\ts_nop 1" :: "v"(p), "v"(v_) : "memory"); } while (0)
#define ST16A ST16_NT
#define ST16B ST16_PLAIN
#define WAITV_BAR(N) asm volatile("s_waitcnt vmcnt(" #N ") lgkmcnt(0)\n\ts_barrier" ::: "memory")

constexpr int P_STAGE = 32768, P_NST = 4, P_EXTRA = P_NST * P_STAGE, P_GW = P_EXTRA + 16384, P_WO = P_GW + 1024, P_LDS = P_WO + 4096;

__device__ __forceinline__ unsigned wswz(unsigned row) { return ((row & 3) << 2) | ((row >> 2) & 3); }
#define LBAR() asm volatile("s_waitcnt lgkmcnt(0)\n\ts_barrier" ::: "memory")

#define PROJ_CODE_LINES 56
#define ATTN_CODE_LINES 64
#define TOUCH_KC 2
template <bool VT>
__device__ __forceinline__ void proj_loop(f32x16 (&acc)[2][2], lds_cptr L, const unsigned (&xoff)[2][4], const unsigned (&woff)[2][2],
                                          bool do_touch, unsigned& touch, const char* dst, int dm, int dc, int du) {
    bf16x8 xf[2][2], wf[2][2];
#define PMM(m, c, buf) acc[m][c] = VT ? MFMA32(xf[buf][m], wf[buf][c], acc[m][c]) : MFMA32(wf[buf][c], xf[buf][m], acc[m][c])
#define PRD(S, s, buf, i) do { xf[buf][i] = ld128(S + xoff[i][s]); wf[buf][i] = cat8(ldtr(S + woff[i][0] + (s) * 4096), ldtr(S + woff[i][1] + (s) * 4096)); } while (0)
    LBAR();
    PRD(L, 0, 0, 0); PRD(L, 0, 0, 1);
#pragma unroll 1
    for (int kc = 0; kc < 8; ++kc) {
        lds_cptr S = L + (kc & 3) * P_STAGE;
        lds_cptr SN = L + ((kc + 1) & 3) * P_STAGE;
        if (kc == TOUCH_KC && do_touch) {
#pragma unroll
            for (int i = 0; i < 8; ++i)
                asm volatile("global_load_dword %0, %1, off" : "+v"(touch) : "v"(dst + (i >> 2) * dm + ((i >> 1) & 1) * dc + (i & 1) * du) : "memory");
        }
#pragma unroll
        for (int s = 0; s < 4; ++s) {
            SBAR();
            if (s == 3 && kc < 7) LBAR();
            PMM(0, 0, s & 1);
            SBAR();
            if (s < 3) PRD(S, s + 1, (s + 1) & 1, 0); else PRD(SN, 0, 0, 0);
            SBAR();
            PMM(0, 1, s & 1);
            SBAR();
            if (s < 3) PRD(S, s + 1, (s + 1) & 1, 1); else PRD(SN, 0, 0, 1);
            SBAR();
            PMM(1, 0, s & 1);
            PMM(1, 1, s & 1);
        }
    }
#undef PMM
#undef PRD
}

__global__ __launch_bounds__(640) void k_proj(const u16* __restrict__ x, const u16* __restrict__ wq, const u16* __restrict__ wk,
                                                 const u16* __restrict__ wv, const u16* __restrict__ wqkv, const u16* __restrict__ woA,
                                                 const u16* __restrict__ woB, const u16* __restrict__ gw, char* __restrict__ ws) {
    extern __shared__ __attribute__((aligned(16))) char smem[];
    const int tid = threadIdx.x, lane = tid & 63, wid = __builtin_amdgcn_readfirstlane(tid >> 6);
    const int bid = blockIdx.x, grp = bid & 7, idx = bid >> 3;
    unsigned ctouch = 0;
    if (wid >= 8 && idx == 0) {
        unsigned long long pc; int rel0, rel1, rel2;
        asm volatile("s_getpc_b64 %0\n\ts_mov_b32 %1, _Z6k_projPKtS0_S0_S0_S0_S0_S0_S0_Pc@rel32@lo+4\n\ts_mov_b32 %2, _Z6k_attnPcPt@rel32@lo+12\n\t"
                     "s_mov_b32 %3, _Z6k_attnPcPt.kd@rel32@lo+20"
                     : "=s"(pc), "=s"(rel0), "=s"(rel1), "=s"(rel2));
        const char* cp = (const char*)pc + (wid == 8 ? rel0 : rel1) + lane * 128;
        if (lane < (wid == 8 ? PROJ_CODE_LINES : ATTN_CODE_LINES))
            asm volatile("global_load_dword %0, %1, off" : "+v"(ctouch) : "v"(cp) : "memory");
    }
    const int cj = idx >> 2;
    const int bi = (grp >> 1) * 4 + (idx & 3);
    const int bj = (cj >> 1) * 4 + (grp & 1) * 2 + (cj & 1);
    const unsigned lds0 = (unsigned)(uintptr_t)smem;
    lds_cptr L = (lds_cptr)smem;
    char* kvbase = ws + WS_KV;
    const u32x4 ones = {0x3f803f80u, 0x3f803f80u, 0x3f803f80u, 0x3f803f80u};
    STAMP_DECL; STAMP(0);
    if (wid >= 8) {
        const int erow = (bj >> 1) * 16 + (lane & 15), eg = lane >> 4;
        const unsigned ex0 = erow * 128 + 16 * (eg ^ ((erow >> 1) & 7)), ex1 = erow * 128 + 16 * ((4 + eg) ^ ((erow >> 1) & 7));
        lds_cptr ewp = L + P_EXTRA + (8 * eg + ((lane & 15) >> 2)) * 32 + 8 * (lane & 3);
        lds_cptr gwp = L + P_GW + eg * 16;
        f32x4 eacc = {};
        __builtin_amdgcn_s_setprio(3);
#pragma unroll 1
        for (int kc = 0; kc < 8; ++kc) {
            LBAR();
            lds_cptr S = L + (kc & 3) * P_STAGE;
            if (wid == 8) {
                eacc = MFMA16(ld128(S + ex0), cat8(ldtr(ewp + kc * 2048), ldtr(ewp + kc * 2048 + 128)), eacc);
                eacc = MFMA16(ld128(S + ex1), cat8(ldtr(ewp + kc * 2048 + 1024), ldtr(ewp + kc * 2048 + 1152)), eacc);
            } else {
                eacc = MFMA16(ld128(S + ex0), ld128(gwp + kc * 128), eacc);
                eacc = MFMA16(ld128(S + ex1), ld128(gwp + kc * 128 + 64), eacc);
            }
        }
        asm volatile("s_waitcnt vmcnt(0)" : "+v"(ctouch) :: "memory");
        const int key0 = bi * 128 + (bj >> 1) * 16;
        if (wid == 8) {
            const unsigned e0 = cvtpk(eacc[0], eacc[1]), e1 = cvtpk(eacc[2], eacc[3]);
            char* tb = kvbase + (size_t)(key0 >> 5) * KV_TILE;
            const int u = (key0 >> 4) & 1, d = lane & 15;
            if (bj & 1) {
                *(u32x2*)(tb + 1024 + u * VBLK + (eg >> 1) * 256 + d * 16 + (eg & 1) * 8) = (u32x2){e0, e1};
                if (lane < 2) *(u32x4*)(tb + 1024 + u * VBLK + 512 + lane * 16) = lane ? (u32x4){0u, 0u, 0u, 0u} : ones;
            } else {
                u16* kb = (u16*)(tb + (d >> 3) * 512 + (u * 16 + 4 * eg) * 16 + (d & 7) * 2);
                kb[0] = (u16)e0; kb[8] = (u16)(e0 >> 16); kb[16] = (u16)e1; kb[24] = (u16)(e1 >> 16);
            }
        } else {
            if (!(bj & 1) && (lane & 15) == 0) {
                f32x4 g, og;
#pragma unroll
                for (int i = 0; i < 4; ++i) { g[i] = rbf(__builtin_amdgcn_rcpf(1.f + __expf(-rbf(eacc[i])))); og[i] = rbf(1.f - g[i]); }
                float* G = (float*)(ws + WS_G);
                *(f32x4*)(G + key0 + 4 * eg) = g; *(f32x4*)(G + SEQ + key0 + 4 * eg) = og;
            }
            if (bid == 0) *(unsigned*)(ws + WS_CNT + lane * 256) = 0u;
        }
        STAMP(6); STAMP_OUT(0);
        return;
    }
    if (wid >= 4) {
        const int lw = wid - 4;
        const u16* W; int ldw, col0;
        if (bj < 4) { W = wq; ldw = 512; col0 = bj * 128; } else { W = wqkv; ldw = 1536; col0 = (bj - 4) * 128; }
        const u16* xs; const u16* wsrc;
        {
            const int row = 8 * lw + (lane >> 3), ch = (lane & 7) ^ ((row >> 1) & 7);
            xs = x + (size_t)(bi * 128 + row) * DM + ch * 8;
            const int kr = 4 * lw + (lane >> 4), wch = (lane & 15) ^ wswz(kr);
            wsrc = W + (size_t)kr * ldw + col0 + wch * 8;
        }
#define PSTAGE(s) do { const unsigned base_ = lds0 + ((s) & (P_NST - 1)) * P_STAGE + lw * 1024; \
        _Pragma("unroll") for (int i = 0; i < 4; ++i) { glds16(xs + (s) * 64 + i * 32 * DM, base_ + i * 4096); glds16(wsrc + ((s) * 64 + i * 16) * ldw, base_ + 16384 + i * 4096); } } while (0)
        PSTAGE(0);
        {
            const u16* we = (bj & 1) ? wv : wk;
#pragma unroll
            for (int i = 0; i < 4; ++i) glds16(we + (lw + 4 * i) * 512 + lane * 8, lds0 + P_EXTRA + (lw + 4 * i) * 1024);
            glds16(gw + lane * 8, lds0 + P_GW);
        }
        PSTAGE(1);
        STAMP(4);
        WAITV_BAR(8);
        PSTAGE(2);
#pragma unroll 1
        for (int kc = 1; kc < 6; ++kc) {
            WAITV_BAR(8);
            PSTAGE(kc + 2);
        }
        {
            const int ks = bid >> 2, kr = 4 * lw + (lane >> 4);
            const u16* wo = (ks < 32) ? woA + (size_t)ks * 16 * DM : woB + (size_t)(ks - 32) * 16 * DM;
            glds16(wo + (size_t)kr * DM + (bid & 3) * 128 + ((lane & 15) ^ wswz(kr)) * 8, lds0 + P_WO + lw * 1024);
        }
        WAITV_BAR(9);
        WAITV_BAR(0);
        {
            const int h = lane >> 5, blk = (lane >> 4) & 1, q = (lane & 15) >> 2, p = lane & 3;
            const int kr0 = 8 * h + q, kr1 = kr0 + 4, ch = 4 * lw + 2 * blk + (p >> 1);
            lds_cptr wb = L + P_WO + 8 * (p & 1);
            *(bf16x8*)(ws + WS_WO + ((size_t)((bid >> 2) * 16 + (bid & 3) * 4 + lw) * 64 + lane) * 16) =
                cat8(ldtr(wb + 256 * kr0 + 16 * (ch ^ wswz(kr0))), ldtr(wb + 256 * kr1 + 16 * (ch ^ wswz(kr1))));
        }
        STAMP(5); STAMP_OUT(0);
        return;
    }
    const int wr = wid >> 1, wc = wid & 1, r = lane & 31, h = lane >> 5;
    f32x16 acc[2][2] = {};
    unsigned xoff[2][4], woff[2][2];
#pragma unroll
    for (int m = 0; m < 2; ++m)
#pragma unroll
        for (int s = 0; s < 4; ++s) { const int row = wr * 64 + m * 32 + r; xoff[m][s] = row * 128 + 16 * ((2 * s + h) ^ ((row >> 1) & 7)); }
    {
        const int blk = (lane >> 4) & 1, q = (lane & 15) >> 2, p = lane & 3;
#pragma unroll
        for (int t = 0; t < 2; ++t) {
            const int krow = 8 * h + 4 * t + q;
#pragma unroll
            for (int c = 0; c < 2; ++c)
                woff[c][t] = 16384 + 256 * krow + 16 * ((4 * (wc * 2 + c) + 2 * blk + (p >> 1)) ^ wswz(krow)) + 8 * (p & 1);
        }
    }
    char* dst; int dm, dc, du;
    {
        const int srow0 = bi * 128 + wr * 64, hd0 = (bj & 3) * 8 + wc * 4;
        if (bj < 8) {
            dst = ws + WS_Q + (size_t)(((bj >> 2) * 32 + hd0) * SEQ + srow0) * 32 + lane * 16;
            dm = 32 * 32; du = SEQ * 32; dc = 2 * du;
        } else if (bj < 12) {
            dst = kvbase + (size_t)(1 + hd0) * KV_HEAD + (srow0 >> 5) * KV_TILE + h * 512 + r * 16;
            dm = (int)KV_TILE; du = (int)KV_HEAD; dc = 2 * du;
        } else {
            dst = kvbase + (size_t)(1 + hd0 + (r >> 4)) * KV_HEAD + (srow0 >> 5) * KV_TILE + 1024 + h * 256 + (r & 15) * 16;
            dm = (int)KV_TILE; du = VBLK; dc = 2 * (int)KV_HEAD;
        }
    }
    unsigned touch = 0;
    const bool do_touch = (bj < 8) ? (lane & 7) == 0 : ((idx & 3) == 0 && (lane & 3) == 0);
    STAMP(1);
    if (bj >= 12) proj_loop<true>(acc, L, xoff, woff, do_touch, touch, dst, dm, dc, du);
    else proj_loop<false>(acc, L, xoff, woff, do_touch, touch, dst, dm, dc, du);
    STAMP(2);
    asm volatile("s_waitcnt vmcnt(0)" : "+v"(touch) :: "memory");
    const float sc = (bj < 8) ? QSCALE : 1.f;
#pragma unroll
    for (int m = 0; m < 2; ++m)
#pragma unroll
        for (int c = 0; c < 2; ++c) {
            const f32x16 a = acc[m][c];
            unsigned w[8];
#pragma unroll
            for (int i = 0; i < 8; ++i) w[i] = cvtpk(a[2 * i] * sc, a[2 * i + 1] * sc);
#pragma unroll
            for (int u = 0; u < 2; ++u) {
                auto s0 = __builtin_amdgcn_permlane32_swap(w[4 * u + 0], w[4 * u + 2], false, false);
                auto s1 = __builtin_amdgcn_permlane32_swap(w[4 * u + 1], w[4 * u + 3], false, false);
                ST16A(dst + m * dm + c * dc + u * du, ((u32x4){s0[0], s1[0], s0[1], s1[1]}));
            }
        }
    if (bj >= 12 && (r & 15) < 2) {
        char* vb = dst - h * 256 - (r & 15) * 16 + h * VBLK + 512 + (r & 1) * 16;
        const u32x4 cv = (r & 1) ? (u32x4){0u, 0u, 0u, 0u} : ones;
#pragma unroll
        for (int m = 0; m < 2; ++m)
#pragma unroll
            for (int c = 0; c < 2; ++c) *(u32x4*)(vb + m * dm + c * dc) = cv;
    }
    STAMP(3); STAMP_OUT(0);
}

constexpr int A_RED = (int)KV_HEAD;
constexpr int A_GATE = A_RED + 24576, A_SYNC = A_GATE + 2048, A_LDS = A_SYNC + 64;

#define PIN(x) asm volatile("" : "+v"(x))
#define SMF(acc, vf, p) do { acc = __builtin_amdgcn_smfmac_f32_32x32x32_bf16(vf, __builtin_bit_cast(bf16x16, p), acc, sidx, 0, 0); PIN(acc); } while (0)
#define EXPR(S, lo, hi) _Pragma("unroll") for (int e_ = lo; e_ < hi; ++e_) S[e_] = __builtin_amdgcn_exp2f(S[e_])
#define LO8(S) __builtin_shufflevector(S, S, 0, 1, 2, 3, 4, 5, 6, 7)
#define HI8(S) __builtin_shufflevector(S, S, 8, 9, 10, 11, 12, 13, 14, 15)

__device__ __forceinline__ float max16(const f32x16& s) {
    float a = fmaxf(fmaxf(s[0], s[1]), s[2]), b = fmaxf(fmaxf(s[3], s[4]), s[5]);
    a = fmaxf(fmaxf(a, s[6]), s[7]); b = fmaxf(fmaxf(b, s[8]), s[9]);
    a = fmaxf(fmaxf(a, s[10]), s[11]); b = fmaxf(fmaxf(b, s[12]), s[13]);
    a = fmaxf(fmaxf(a, s[14]), s[15]);
    return fmaxf(a, b);
}
__device__ __forceinline__ float xhalf_max(float m) {
    auto rr = __builtin_amdgcn_permlane32_swap(__float_as_uint(m), __float_as_uint(m), false, false);
    return fmaxf(__uint_as_float(rr[0]), __uint_as_float(rr[1]));
}

#define ATT_STEP(KN, V1C, V0N) do { \
    SBAR(); EXPR(SA, 0, 2);   SBAR(); SMF(OB, v1, HI8(SB)); v1 = ld128(V1C); \
    SBAR(); EXPR(SA, 2, 6);   SBAR(); SB = MFMA32(kf, qbf, CB); PIN(SB); kf = ld128(KN); \
    SBAR(); EXPR(SA, 6, 10);  SBAR(); SMF(OA, v0, LO8(SA)); \
    SBAR(); EXPR(SA, 10, 16); EXPR(SB, 0, 2); SBAR(); SMF(OA, v1, HI8(SA)); \
    SBAR(); EXPR(SB, 2, 6);   SBAR(); SA = MFMA32(kf, qa, CA); PIN(SA); \
    SBAR(); EXPR(SB, 6, 10);  SBAR(); SMF(OB, v0, LO8(SB)); v0 = ld128(V0N); \
    SBAR(); EXPR(SB, 10, 16); } while (0)

#define POLL_SLEEP 4
#define LATE_DELAY 8
#define PRIO_C 5
typedef __attribute__((address_space(1))) unsigned gu32;
typedef __attribute__((address_space(3))) unsigned lu32;
__device__ __forceinline__ unsigned lds_ld(lu32* p) { return __builtin_amdgcn_readfirstlane(__hip_atomic_load(p, __ATOMIC_RELAXED, __HIP_MEMORY_SCOPE_WORKGROUP)); }
__device__ __forceinline__ void lds_arrive_wait(lu32* w, unsigned target, int lane) {
    asm volatile("s_waitcnt lgkmcnt(0)" ::: "memory");
    if (lane == 0) __hip_atomic_fetch_add(w, 1u, __ATOMIC_RELAXED, __HIP_MEMORY_SCOPE_WORKGROUP);
#pragma unroll 1
    for (unsigned spins = 0; spins < (1u << 22); ++spins) { if (lds_ld(w) >= target) break; __builtin_amdgcn_s_sleep(1); }
    asm volatile("" ::: "memory");
}

__global__ __launch_bounds__(512, 2) void k_attn(char* __restrict__ ws, u16* __restrict__ out) {
    extern __shared__ __attribute__((aligned(16))) char smem[];
    const int tid = threadIdx.x, lane = tid & 63, wid = __builtin_amdgcn_readfirstlane(tid >> 6);
    const int bid = blockIdx.x, grp = bid & 7, idx = bid >> 3;
    const int head = (idx >> 4) * 32 + (grp & 1) * 16 + (idx & 15), qb = grp >> 1;
    const int ort = idx & 7, obn = (grp & 1) * 4 + (idx >> 3);
    const int kvh = head < 32 ? 0 : head - 31;
    const unsigned lds0 = (unsigned)(uintptr_t)smem;
    const int q0 = qb * 512 + wid * 64;
    STAMP_DECL; STAMP(0);
    const int r = lane & 31, h = lane >> 5;
    bf16x8 qa, qbf;
    {
        const char* qsrc = ws + WS_Q + (size_t)(head * SEQ + q0) * 32 + lane * 16;
        asm volatile("global_load_dwordx4 %0, %2, off\n\tglobal_load_dwordx4 %1, %2, off offset:1024"
                     : "=&v"(qa), "=&v"(qbf) : "v"(qsrc) : "memory");
    }
    const char* ksrc = ws + WS_KV + (size_t)kvh * KV_HEAD + lane * 16;
#define KVDMA(p) do { int p_ = qb * 33 + (p); p_ = p_ >= 132 ? p_ - 132 : p_; glds16(ksrc + p_ * 1024, lds0 + (p) * 1024); } while (0)
    KVDMA(wid);
    STAMP(14);
    const __amdgpu_buffer_rsrc_t rsO = __builtin_amdgcn_make_buffer_rsrc((void*)(ws + WS_O), 0, 64 * SEQ * DH * 2, 0x00020000);
    const unsigned ooff = (unsigned)(head * SEQ + q0) * 32 + lane * 16;
    const u32x4 poison = {~0u, ~0u, ~0u, ~0u};
    lds_cptr L = (lds_cptr)smem;
    lu32* sync = (lu32*)(smem + A_SYNC);
    if (tid < 16) sync[tid] = 0u;
    lds_cptr kp = L + lane * 16;
    lds_cptr vp = L + 1024 + (r < 16 ? h * 256 + r * 16 : ((r == 16 || r == 20) ? 512 : 528));
    int sidx = 0xDDDD; PIN(sidx);
    f32x16 OA, OB, CA, CB, SA, SB;
#pragma unroll
    for (int i = 0; i < 16; ++i) { OA[i] = 0.f; OB[i] = 0.f; SB[i] = 0.f; }
    asm volatile("" : "+v"(OA), "+v"(OB), "+v"(SB));
    asm volatile("s_waitcnt vmcnt(0) lgkmcnt(0)\n\ts_barrier" : "+v"(qa), "+v"(qbf) :: "memory");
#define LANDED(w) do { if (lane == 0) __hip_atomic_fetch_add(sync + 8 + (w), 1u, __ATOMIC_RELAXED, __HIP_MEMORY_SCOPE_WORKGROUP); } while (0)
#define AWAIT(w) do { SBAR(); _Pragma("unroll 1") for (unsigned sp_ = 0; sp_ < (1u << 22); ++sp_) { if (lds_ld(sync + 8 + (w)) >= 4u) break; __builtin_amdgcn_s_sleep(1); } \
                      asm volatile("" ::: "memory"); SBAR(); } while (0)
    if (wid >= 4) {
#pragma unroll 1
        for (int i = 0; i < 8; ++i) KVDMA(4 + wid + 4 * i);
        asm volatile("s_waitcnt vmcnt(4)" ::: "memory"); LANDED(0);
#pragma unroll 1
        for (int i = 8; i < 16; ++i) KVDMA(4 + wid + 4 * i);
        asm volatile("s_waitcnt vmcnt(8)" ::: "memory"); LANDED(1);
#pragma unroll 1
        for (int i = 16; i < 24; ++i) KVDMA(4 + wid + 4 * i);
        asm volatile("s_waitcnt vmcnt(8)" ::: "memory"); LANDED(2);
#pragma unroll 1
        for (int i = 24; i < 31; ++i) KVDMA(4 + wid + 4 * i);
        if (wid < 6) glds16(ws + WS_G + ((head < 32 ? 0 : SEQ) + qb * 512) * 4 + (wid - 4) * 1024 + lane * 16, lds0 + A_GATE + (wid - 4) * 1024);
        __builtin_amdgcn_raw_buffer_store_b128(poison, rsO, ooff - 8192, 0, 16); __builtin_amdgcn_raw_buffer_store_b128(poison, rsO, ooff - 7168, 0, 16);
        __builtin_amdgcn_raw_buffer_store_b128(poison, rsO, ooff, 0, 16); __builtin_amdgcn_raw_buffer_store_b128(poison, rsO, ooff + 1024, 0, 16);
        asm volatile("s_waitcnt vmcnt(0)" ::: "memory");
        unsigned prev = 0;
        if (lane == 0) prev = __hip_atomic_fetch_add(sync + 8 + 3, 1u, __ATOMIC_RELAXED, __HIP_MEMORY_SCOPE_WORKGROUP);
        if (__builtin_amdgcn_readfirstlane(prev) == 3u && lane < 8)
            __hip_atomic_fetch_add((gu32*)(ws + WS_CNT + (qb * 8 + lane) * 256), 1u, __ATOMIC_RELAXED, __HIP_MEMORY_SCOPE_AGENT);
        AWAIT(3);
        __builtin_amdgcn_s_setprio(1);
    }
    STAMP(1);
    float ma, mb;
    bf16x8 kf, v0, v1;
    {
        const f32x16 z = {};
        kf = ld128(kp);
        const bf16x8 k1 = ld128(kp + KV_TILE), k2 = ld128(kp + 2 * KV_TILE), k3 = ld128(kp + 3 * KV_TILE);
        v0 = ld128(vp); v1 = ld128(vp + VBLK);
        const f32x16 s0 = MFMA32(kf, qa, z), s1 = MFMA32(k1, qa, z), s2 = MFMA32(kf, qbf, z), s3 = MFMA32(k1, qbf, z);
        ma = fmaxf(max16(s0), max16(s1)); mb = fmaxf(max16(s2), max16(s3));
        const f32x16 s4 = MFMA32(k2, qa, z), s5 = MFMA32(k3, qa, z), s6 = MFMA32(k2, qbf, z), s7 = MFMA32(k3, qbf, z);
        ma = xhalf_max(fmaxf(ma, fmaxf(max16(s4), max16(s5))));
        mb = xhalf_max(fmaxf(mb, fmaxf(max16(s6), max16(s7))));
    }
    for (int attempt = 0; attempt < 2; ++attempt) {
#pragma unroll
        for (int i = 0; i < 16; ++i) { CA[i] = -ma; CB[i] = -mb; }
        asm volatile("" : "+v"(CA), "+v"(CB));
        SA = MFMA32(kf, qa, CA);
        if (attempt == 0) STAMP(2);
#pragma unroll 1
        for (int c = 0; c < 8; ++c) {
            if (c == PRIO_C) __builtin_amdgcn_s_setprio(0);
            if (c < 3 || c == 4) AWAIT(c < 3 ? c : 3);
            lds_cptr kc = kp + c * 8 * KV_TILE;
            lds_cptr vc = vp + c * 8 * KV_TILE;
#pragma unroll
            for (int t = 0; t < 8; ++t) {
                ATT_STEP(kc + (t + 1) * KV_TILE, vc + t * KV_TILE + VBLK, vc + (t + 1) * KV_TILE);
            }
        }
        SBAR();
        SMF(OB, v1, HI8(SB));
        if (attempt == 0) STAMP(3); else STAMP(5);
        const float la = OA[8], lb = OB[8];
        float oa = 0.f, ob = 0.f;
#pragma unroll
        for (int i = 0; i < 8; ++i) { oa += fabsf(OA[i]); ob += fabsf(OB[i]); }
        const bool ok = (la <= 0x1p125f) && (lb <= 0x1p125f) && (oa <= 0x1p126f) && (ob <= 0x1p126f);
        if (attempt == 1 || __all(ok)) break;
        const f32x16 z = {};
        float ta = -INFINITY, tb = -INFINITY;
#pragma unroll 1
        for (int t = 0; t < 64; ++t) {
            const bf16x8 k0 = ld128(kp + t * KV_TILE);
            ta = fmaxf(ta, max16(MFMA32(k0, qa, z)));
            tb = fmaxf(tb, max16(MFMA32(k0, qbf, z)));
        }
        ma = xhalf_max(ta); mb = xhalf_max(tb);
#pragma unroll
        for (int i = 0; i < 16; ++i) { OA[i] = 0.f; OB[i] = 0.f; SB[i] = 0.f; }
        kf = ld128(kp); v0 = ld128(vp); v1 = ld128(vp + VBLK);
    }
    {
        const float* gl = (const float*)(smem + A_GATE) + wid * 64 + r;
        const float sa = gl[0] * __builtin_amdgcn_rcpf(OA[8]), sb = gl[32] * __builtin_amdgcn_rcpf(OB[8]);
        unsigned w[4];
#pragma unroll
        for (int i = 0; i < 4; ++i) w[i] = min(cvtpk(OA[2 * i] * sa, OA[2 * i + 1] * sa), 0xfffffffeu);
        auto s0 = __builtin_amdgcn_permlane32_swap(w[0], w[2], false, false);
        auto s1 = __builtin_amdgcn_permlane32_swap(w[1], w[3], false, false);
        __builtin_amdgcn_raw_buffer_store_b128((u32x4){s0[0], s1[0], s0[1], s1[1]}, rsO, ooff, 0, 16);
#pragma unroll
        for (int i = 0; i < 4; ++i) w[i] = min(cvtpk(OB[2 * i] * sb, OB[2 * i + 1] * sb), 0xfffffffeu);
        auto t0 = __builtin_amdgcn_permlane32_swap(w[0], w[2], false, false);
        auto t1 = __builtin_amdgcn_permlane32_swap(w[1], w[3], false, false);
        __builtin_amdgcn_raw_buffer_store_b128((u32x4){t0[0], t1[0], t0[1], t1[1]}, rsO, ooff + 1024, 0, 16);
        if (wid >= 4) {
            if (lane == 0) __hip_atomic_fetch_add(sync + 1, 1u, __ATOMIC_RELAXED, __HIP_MEMORY_SCOPE_WORKGROUP);
            STAMP(4); STAMP_OUT(1);
            return;
        }
    }
    STAMP(4);
    const int bn = obn, kq = wid;
    bf16x8 wf[16][2];
    {
        const int d0 = (kq + (r >> 4)) & 3, d1 = (kq + 2 + (r >> 4)) & 3;
        const char* Wp = ws + WS_WO + ((size_t)(kq * 16) * 16 + bn * 2) * 1024 + (h * 32 + (r & 15)) * 16;
        const char* Wp0 = Wp + (d0 >> 1) * 1024 + (d0 & 1) * 256;
        const char* Wp1 = Wp + (d1 >> 1) * 1024 + (d1 & 1) * 256;
#pragma unroll
        for (int k = 0; k < 16; ++k) { wf[k][0] = *(const bf16x8*)(Wp0 + k * 16384); wf[k][1] = *(const bf16x8*)(Wp1 + k * 16384); }
    }
    {
        gu32* cnt = (gu32*)(ws + WS_CNT + (qb * 8 + ort) * 256);
#pragma unroll 1
        for (unsigned spins = 0; spins < (1u << 20); ++spins) {
            if ((unsigned)__builtin_amdgcn_readfirstlane(__hip_atomic_load(cnt, __ATOMIC_RELAXED, __HIP_MEMORY_SCOPE_AGENT)) >= 64u) break;
            __builtin_amdgcn_s_sleep(8);
        }
        asm volatile("" ::: "memory");
    }
    f32x4* red = (f32x4*)(smem + A_RED);
#pragma unroll 1
    for (int ph = 0; ph < 2; ++ph) {
        const int row0 = qb * 512 + ph * 256 + ort * 32;
        const unsigned aoff = (unsigned)(kq * 16 * SEQ + row0) * 32 + lane * 16;
        if (ph == 1) {
#pragma unroll 1
            for (unsigned spins = 0; spins < (1u << 24); ++spins) { if (lds_ld(sync + 1) >= 4u) break; __builtin_amdgcn_s_sleep(4); }
            __builtin_amdgcn_s_sleep(LATE_DELAY);
        }
        if (ph == 0) STAMP(7); else STAMP(10);
        f32x16 acc[2];
#pragma unroll 1
        for (unsigned spins = 0; spins < (1u << 20); ++spins) {
            asm volatile("" ::: "memory");
            u32x4 of[16];
#pragma unroll
            for (int k = 0; k < 16; ++k) of[k] = __builtin_amdgcn_raw_buffer_load_b128(rsO, aoff, k * SEQ * 32, 16);
            SBAR();
            acc[0] = (f32x16){}; acc[1] = (f32x16){};
            unsigned mx = 0;
#pragma unroll
            for (int k = 0; k < 16; ++k) {
                acc[0] = MFMA32(wf[k][0], __builtin_bit_cast(bf16x8, of[k]), acc[0]);
                acc[1] = MFMA32(wf[k][1], __builtin_bit_cast(bf16x8, of[k]), acc[1]);
                mx = max(max(mx, max(of[k][0], of[k][1])), max(of[k][2], of[k][3]));
                PIN(mx); SBAR();
            }
            if (!__any(mx == ~0u)) break;
            __builtin_amdgcn_s_sleep(POLL_SLEEP);
        }
        if (ph == 0) STAMP(8); else STAMP(11);
        f32x4 own[2];
#pragma unroll
        for (int j = 0; j < 4; ++j)
#pragma unroll
            for (int g = 0; g < 2; ++g) {
                const int e = 4 * (2 * (j & 1) + g);
                const f32x4 v = {acc[j >> 1][e], acc[j >> 1][e + 1], acc[j >> 1][e + 2], acc[j >> 1][e + 3]};
                if (j == 0) own[g] = v;
                else red[((((kq + j) & 3) * 3 + j - 1) * 2 + g) * 64 + lane] = v;
            }
        if (ph == 0) lds_arrive_wait(sync + 4, 4u, lane);
        else LBAR();
#pragma unroll
        for (int sl = 0; sl < 3; ++sl)
#pragma unroll
            for (int g = 0; g < 2; ++g) own[g] += red[((kq * 3 + sl) * 2 + g) * 64 + lane];
        auto s0 = __builtin_amdgcn_permlane32_swap(cvtpk(own[0][0], own[0][1]), cvtpk(own[1][0], own[1][1]), false, false);
        auto s1 = __builtin_amdgcn_permlane32_swap(cvtpk(own[0][2], own[0][3]), cvtpk(own[1][2], own[1][3]), false, false);
        ST16B(out + (size_t)(row0 + r) * DM + bn * 64 + kq * 16 + h * 8, ((u32x4){s0[0], s1[0], s0[1], s1[1]}));
        if (ph == 0) { lds_arrive_wait(sync + 5, 4u, lane); STAMP(9); }
    }
    STAMP(12); STAMP_OUT(1);
}

extern "C" void kernel_launch(void* const* d_in, const int* in_sizes, int n_in,
                              void* d_out, int out_size, void* d_ws, size_t ws_size,
                              hipStream_t stream) {
    const u16* x = (const u16*)d_in[0];
    const u16* wq = (const u16*)d_in[1];
    const u16* wk = (const u16*)d_in[2];
    const u16* wv = (const u16*)d_in[3];
    const u16* woA = (const u16*)d_in[4];
    const u16* wqkv = (const u16*)d_in[6];
    const u16* woB = (const u16*)d_in[7];
    const u16* gw = (const u16*)d_in[8];
    char* ws = (char*)d_ws;
    (void)hipFuncSetAttribute((const void*)k_proj, hipFuncAttributeMaxDynamicSharedMemorySize, P_LDS);
    (void)hipFuncSetAttribute((const void*)k_attn, hipFuncAttributeMaxDynamicSharedMemorySize, A_LDS);
    k_proj<<<256, 640, P_LDS, stream>>>(x, wq, wk, wv, wqkv, woA, woB, gw, ws);
    k_attn<<<256, 512, A_LDS, stream>>>(ws, (u16*)d_out);
}
```

```cpp
#include <hip/hip_runtime.h>
#include <hip/hip_bf16.h>
#include <stdint.h>

typedef __attribute__((ext_vector_type(8))) __bf16 bf16x8;
typedef __attribute__((ext_vector_type(16))) __bf16 bf16x16;
typedef __attribute__((ext_vector_type(4))) short s16x4;
typedef __attribute__((ext_vector_type(8))) short s16x8;
typedef __attribute__((ext_vector_type(4))) float f32x4;
typedef __attribute__((ext_vector_type(8))) float f32x8;
typedef __attribute__((ext_vector_type(16))) float f32x16;
typedef __attribute__((ext_vector_type(4))) unsigned u32x4;
typedef __attribute__((ext_vector_type(2))) unsigned u32x2;
typedef unsigned short u16;
typedef __attribute__((address_space(3))) const char* lds_cptr;
typedef __attribute__((address_space(3))) char* lds_ptr;

constexpr int SEQ = 2048, DM = 512, NH = 32, DH = 16;
constexpr float QSCALE = 0.25f * 1.4426950408889634f;

constexpr int VBLK = 544;
constexpr size_t KV_TILE = 1024 + 2 * VBLK;
constexpr size_t KV_HEAD = 64 * KV_TILE;
constexpr size_t WS_Q = 0;
constexpr size_t WS_O = WS_Q + (size_t)64 * SEQ * DH * 2;
constexpr size_t WS_WO = WS_O + (size_t)64 * SEQ * DH * 2;
constexpr size_t WS_G = WS_WO + (size_t)1024 * 512 * 2;
constexpr size_t WS_CNT = WS_G + 2 * SEQ * 4;
constexpr size_t WS_KV = WS_CNT + 64 * 256;
constexpr size_t WS_END = WS_KV + 34 * KV_HEAD;

#define SBAR() __builtin_amdgcn_sched_barrier(0)
#define MFMA32(a, b, c) __builtin_amdgcn_mfma_f32_32x32x16_bf16(a, b, c, 0, 0, 0)
#define MFMA16(a, b, c) __builtin_amdgcn_mfma_f32_16x16x32_bf16(a, b, c, 0, 0, 0)

typedef __attribute__((ext_vector_type(2))) __bf16 bf16x2;
typedef __attribute__((ext_vector_type(2))) float f32x2;
__device__ __forceinline__ unsigned cvtpk(float lo, float hi) {
    return __builtin_bit_cast(unsigned, __builtin_convertvector((f32x2){lo, hi}, bf16x2));
}
#define STAMP(i)
#define STAMP_DECL
#define STAMP_OUT(k)
__device__ __forceinline__ float bf2f(u16 v) { return __uint_as_float((unsigned)v << 16); }
__device__ __forceinline__ float rbf(float v) { return __uint_as_float(cvtpk(v, 0.f) << 16); }

__device__ __forceinline__ void glds16(const void* g, unsigned lds_base) {
    unsigned sv;
    asm volatile("s_mov_b32 %0, m0\n\ts_mov_b32 m0, %2\n\ts_nop 0\n\tglobal_load_lds_dwordx4 %1, off\n\ts_mov_b32 m0, %0"
                 : "=&s"(sv) : "v"(g), "s"(lds_base) : "memory");
}
__device__ __forceinline__ s16x4 ldtr(lds_cptr p) {
    typedef short v4i16_t __attribute__((ext_vector_type(4)));
    return __builtin_bit_cast(s16x4, __builtin_amdgcn_ds_read_tr16_b64_v4i16((__attribute__((address_space(3))) v4i16_t*)p));
}
__device__ __forceinline__ bf16x8 ld128(lds_cptr p) { return *(const __attribute__((address_space(3))) bf16x8*)p; }
__device__ __forceinline__ bf16x8 cat8(s16x4 a, s16x4 b) {
    s16x8 v = {a[0], a[1], a[2], a[3], b[0], b[1], b[2], b[3]}; return __builtin_bit_cast(bf16x8, v);
}
#define ST16_PLAIN(p, v) (*(u32x4*)(p) = (v))
#define ST16_NT(p, v) __builtin_nontemporal_store((u32x4)(v), (u32x4*)(p))
#define ST16_SC1(p, v) do { u32x4 v_ = (v); asm volatile("global_store_dwordx4 %0, %1, off sc1\n\ts_nop 1" :: "v"(p), "v"(v_) : "memory"); } while (0)
#define ST16A ST16_NT
#define ST16B ST16_PLAIN
#define WAITV_BAR(N) asm volatile("s_waitcnt vmcnt(" #N ") lgkmcnt(0)\n\ts_barrier" ::: "memory")

constexpr int P_STAGE = 32768, P_NST = 4, P_EXTRA = P_NST * P_STAGE, P_GW = P_EXTRA + 16384, P_WO = P_GW + 1024, P_LDS = P_WO + 4096;

__device__ __forceinline__ unsigned wswz(unsigned row) { return ((row & 3) << 2) | ((row >> 2) & 3); }
#define LBAR() asm volatile("s_waitcnt lgkmcnt(0)\n\ts_barrier" ::: "memory")

#define PROJ_CODE_LINES 56
#define ATTN_CODE_LINES 64
#define ROT 1
#define TOUCH_KC 2
template <bool VT>
__device__ __forceinline__ void proj_loop(f32x16 (&acc)[2][2], lds_cptr L, const unsigned (&xoff)[2][4], const unsigned (&woff)[2][2],
                                          bool do_touch, unsigned& touch, const char* dst, int dm, int dc, int du) {
    bf16x8 xf[2][2], wf[2][2];
#define PMM(m, c, buf) acc[m][c] = VT ? MFMA32(xf[buf][m], wf[buf][c], acc[m][c]) : MFMA32(wf[buf][c], xf[buf][m], acc[m][c])
#define PRD(S, s, buf, i) do { xf[buf][i] = ld128(S + xoff[i][s]); wf[buf][i] = cat8(ldtr(S + woff[i][0] + (s) * 4096), ldtr(S + woff[i][1] + (s) * 4096)); } while (0)
    LBAR();
    PRD(L, 0, 0, 0); PRD(L, 0, 0, 1);
#pragma unroll 1
    for (int kc = 0; kc < 8; ++kc) {
        lds_cptr S = L + (kc & 3) * P_STAGE;
        lds_cptr SN = L + ((kc + 1) & 3) * P_STAGE;
        if (kc == TOUCH_KC && do_touch) {
#pragma unroll
            for (int i = 0; i < 8; ++i)
                asm volatile("global_load_dword %0, %1, off" : "+v"(touch) : "v"(dst + (i >> 2) * dm + ((i >> 1) & 1) * dc + (i & 1) * du) : "memory");
        }
#pragma unroll
        for (int s = 0; s < 4; ++s) {
            SBAR();
            if (s == 3 && kc < 7) LBAR();
            PMM(0, 0, s & 1);
            SBAR();
            if (s < 3) PRD(S, s + 1, (s + 1) & 1, 0); else PRD(SN, 0, 0, 0);
            SBAR();
            PMM(0, 1, s & 1);
            SBAR();
            if (s < 3) PRD(S, s + 1, (s + 1) & 1, 1); else PRD(SN, 0, 0, 1);
            SBAR();
            PMM(1, 0, s & 1);
            PMM(1, 1, s & 1);
        }
    }
#undef PMM
#undef PRD
}

__global__ __launch_bounds__(640) void k_proj(const u16* __restrict__ x, const u16* __restrict__ wq, const u16* __restrict__ wk,
                                                 const u16* __restrict__ wv, const u16* __restrict__ wqkv, const u16* __restrict__ woA,
                                                 const u16* __restrict__ woB, const u16* __restrict__ gw, char* __restrict__ ws) {
    extern __shared__ __attribute__((aligned(16))) char smem[];
    const int tid = threadIdx.x, lane = tid & 63, wid = __builtin_amdgcn_readfirstlane(tid >> 6);
    const int bid = blockIdx.x, grp = bid & 7, idx = bid >> 3;
    unsigned ctouch = 0;
    if (wid >= 8 && idx == 0) {
        unsigned long long pc; int rel0, rel1, rel2;
        asm volatile("s_getpc_b64 %0\n\ts_mov_b32 %1, _Z6k_projPKtS0_S0_S0_S0_S0_S0_S0_Pc@rel32@lo+4\n\ts_mov_b32 %2, _Z6k_attnPcPt@rel32@lo+12\n\t"
                     "s_mov_b32 %3, _Z6k_attnPcPt.kd@rel32@lo+20"
                     : "=s"(pc), "=s"(rel0), "=s"(rel1), "=s"(rel2));
        const char* cp = (const char*)pc + (wid == 8 ? rel0 : rel1) + lane * 128;
        if (lane < (wid == 8 ? PROJ_CODE_LINES : ATTN_CODE_LINES))
            asm volatile("global_load_dword %0, %1, off" : "+v"(ctouch) : "v"(cp) : "memory");
    }
    const int cj = idx >> 2;
    const int bi = (grp >> 1) * 4 + (idx & 3);
    const int bj = (cj >> 1) * 4 + (grp & 1) * 2 + (cj & 1);
    const int rot = grp;
    const unsigned lds0 = (unsigned)(uintptr_t)smem;
    lds_cptr L = (lds_cptr)smem;
    char* kvbase = ws + WS_KV;
    const u32x4 ones = {0x3f803f80u, 0x3f803f80u, 0x3f803f80u, 0x3f803f80u};
    STAMP_DECL; STAMP(0);
    if (wid >= 8) {
        const int erow = (bj >> 1) * 16 + (lane & 15), eg = lane >> 4;
        const unsigned ex0 = erow * 128 + 16 * (eg ^ ((erow >> 1) & 7)), ex1 = erow * 128 + 16 * ((4 + eg) ^ ((erow >> 1) & 7));
        lds_cptr ewp = L + P_EXTRA + (8 * eg + ((lane & 15) >> 2)) * 32 + 8 * (lane & 3);
        lds_cptr gwp = L + P_GW + eg * 16;
        f32x4 eacc = {};
        __builtin_amdgcn_s_setprio(3);
#pragma unroll 1
        for (int kc = 0; kc < 8; ++kc) {
            LBAR();
            lds_cptr S = L + (kc & 3) * P_STAGE;
            const int kg = (kc + rot) & 7;
            if (wid == 8) {
                eacc = MFMA16(ld128(S + ex0), cat8(ldtr(ewp + kg * 2048), ldtr(ewp + kg * 2048 + 128)), eacc);
                eacc = MFMA16(ld128(S + ex1), cat8(ldtr(ewp + kg * 2048 + 1024), ldtr(ewp + kg * 2048 + 1152)), eacc);
            } else {
                eacc = MFMA16(ld128(S + ex0), ld128(gwp + kg * 128), eacc);
                eacc = MFMA16(ld128(S + ex1), ld128(gwp + kg * 128 + 64), eacc);
            }
        }
        asm volatile("s_waitcnt vmcnt(0)" : "+v"(ctouch) :: "memory");
        const int key0 = bi * 128 + (bj >> 1) * 16;
        if (wid == 8) {
            const unsigned e0 = cvtpk(eacc[0], eacc[1]), e1 = cvtpk(eacc[2], eacc[3]);
            char* tb = kvbase + (size_t)(key0 >> 5) * KV_TILE;
            const int u = (key0 >> 4) & 1, d = lane & 15;
            if (bj & 1) {
                *(u32x2*)(tb + 1024 + u * VBLK + (eg >> 1) * 256 + d * 16 + (eg & 1) * 8) = (u32x2){e0, e1};
                if (lane < 2) *(u32x4*)(tb + 1024 + u * VBLK + 512 + lane * 16) = lane ? (u32x4){0u, 0u, 0u, 0u} : ones;
            } else {
                u16* kb = (u16*)(tb + (d >> 3) * 512 + (u * 16 + 4 * eg) * 16 + (d & 7) * 2);
                kb[0] = (u16)e0; kb[8] = (u16)(e0 >> 16); kb[16] = (u16)e1; kb[24] = (u16)(e1 >> 16);
            }
        } else {
            if (!(bj & 1) && (lane & 15) == 0) {
                f32x4 g, og;
#pragma unroll
                for (int i = 0; i < 4; ++i) { g[i] = rbf(__builtin_amdgcn_rcpf(1.f + __expf(-rbf(eacc[i])))); og[i] = rbf(1.f - g[i]); }
                float* G = (float*)(ws + WS_G);
                *(f32x4*)(G + key0 + 4 * eg) = g; *(f32x4*)(G + SEQ + key0 + 4 * eg) = og;
            }
            if (bid == 0) *(unsigned*)(ws + WS_CNT + lane * 256) = 0u;
        }
        STAMP(6); STAMP_OUT(0);
        return;
    }
    if (wid >= 4) {
        const int lw = wid - 4;
        const u16* W; int ldw, col0;
        if (bj < 4) { W = wq; ldw = 512; col0 = bj * 128; } else { W = wqkv; ldw = 1536; col0 = (bj - 4) * 128; }
        const u16* xs; const u16* wsrc;
        {
            const int row = 8 * lw + (lane >> 3), ch = (lane & 7) ^ ((row >> 1) & 7);
            xs = x + (size_t)(bi * 128 + row) * DM + ch * 8;
            const int kr = 4 * lw + (lane >> 4), wch = (lane & 15) ^ wswz(kr);
            wsrc = W + (size_t)kr * ldw + col0 + wch * 8;
        }
#define PSTAGE(s) do { const unsigned base_ = lds0 + ((s) & (P_NST - 1)) * P_STAGE + lw * 1024; const int kc_ = ((s) + rot) & 7; \
        _Pragma("unroll") for (int i = 0; i < 4; ++i) { glds16(xs + kc_ * 64 + i * 32 * DM, base_ + i * 4096); glds16(wsrc + (kc_ * 64 + i * 16) * ldw, base_ + 16384 + i * 4096); } } while (0)
        STAMP(7);
        PSTAGE(0);
        STAMP(8);
        {
            const u16* we = (bj & 1) ? wv : wk;
#pragma unroll
            for (int i = 0; i < 4; ++i) glds16(we + (lw + 4 * i) * 512 + lane * 8, lds0 + P_EXTRA + (lw + 4 * i) * 1024);
            glds16(gw + lane * 8, lds0 + P_GW);
        }
        PSTAGE(1);
        STAMP(4);
        WAITV_BAR(8);
        STAMP(9);
        PSTAGE(2);
#pragma unroll 1
        for (int kc = 1; kc < 6; ++kc) {
            WAITV_BAR(8);
            if (kc == 1) STAMP(10); if (kc == 3) STAMP(11); if (kc == 5) STAMP(12);
            PSTAGE(kc + 2);
        }
        {
            const int ks = bid >> 2, kr = 4 * lw + (lane >> 4);
            const u16* wo = (ks < 32) ? woA + (size_t)ks * 16 * DM : woB + (size_t)(ks - 32) * 16 * DM;
            glds16(wo + (size_t)kr * DM + (bid & 3) * 128 + ((lane & 15) ^ wswz(kr)) * 8, lds0 + P_WO + lw * 1024);
        }
        WAITV_BAR(9);
        WAITV_BAR(0);
        {
            const int h = lane >> 5, blk = (lane >> 4) & 1, q = (lane & 15) >> 2, p = lane & 3;
            const int kr0 = 8 * h + q, kr1 = kr0 + 4, ch = 4 * lw + 2 * blk + (p >> 1);
            lds_cptr wb = L + P_WO + 8 * (p & 1);
            *(bf16x8*)(ws + WS_WO + ((size_t)((bid >> 2) * 16 + (bid & 3) * 4 + lw) * 64 + lane) * 16) =
                cat8(ldtr(wb + 256 * kr0 + 16 * (ch ^ wswz(kr0))), ldtr(wb + 256 * kr1 + 16 * (ch ^ wswz(kr1))));
        }
        STAMP(5); STAMP_OUT(0);
        return;
    }
    unsigned touch = 0;
    const int wr = wid >> 1, wc = wid & 1, r = lane & 31, h = lane >> 5;
    f32x16 acc[2][2] = {};
    unsigned xoff[2][4], woff[2][2];
#pragma unroll
    for (int m = 0; m < 2; ++m)
#pragma unroll
        for (int s = 0; s < 4; ++s) { const int row = wr * 64 + m * 32 + r; xoff[m][s] = row * 128 + 16 * ((2 * s + h) ^ ((row >> 1) & 7)); }
    {
        const int blk = (lane >> 4) & 1, q = (lane & 15) >> 2, p = lane & 3;
#pragma unroll
        for (int t = 0; t < 2; ++t) {
            const int krow = 8 * h + 4 * t + q;
#pragma unroll
            for (int c = 0; c < 2; ++c)
                woff[c][t] = 16384 + 256 * krow + 16 * ((4 * (wc * 2 + c) + 2 * blk + (p >> 1)) ^ wswz(krow)) + 8 * (p & 1);
        }
    }
    char* dst; int dm, dc, du;
    {
        const int srow0 = bi * 128 + wr * 64, hd0 = (bj & 3) * 8 + wc * 4;
        if (bj < 8) {
            dst = ws + WS_Q + (size_t)(((bj >> 2) * 32 + hd0) * SEQ + srow0) * 32 + lane * 16;
            dm = 32 * 32; du = SEQ * 32; dc = 2 * du;
        } else if (bj < 12) {
            dst = kvbase + (size_t)(1 + hd0) * KV_HEAD + (srow0 >> 5) * KV_TILE + h * 512 + r * 16;
            dm = (int)KV_TILE; du = (int)KV_HEAD; dc = 2 * du;
        } else {
            dst = kvbase + (size_t)(1 + hd0 + (r >> 4)) * KV_HEAD + (srow0 >> 5) * KV_TILE + 1024 + h * 256 + (r & 15) * 16;
            dm = (int)KV_TILE; du = VBLK; dc = 2 * (int)KV_HEAD;
        }
    }
    const bool do_touch = (bj < 8) ? (lane & 7) == 0 : ((idx & 3) == 0 && (lane & 3) == 0);
    if ((lane & 47) == 0) {
#pragma unroll
        for (int i = 0; i < 8; ++i)
            asm volatile("global_load_dword %0, %1, off" : "+v"(touch) : "v"(dst + (i >> 2) * dm + ((i >> 1) & 1) * dc + (i & 1) * du) : "memory");
    }
    STAMP(1);
    if (bj >= 12) proj_loop<true>(acc, L, xoff, woff, do_touch, touch, dst, dm, dc, du);
    else proj_loop<false>(acc, L, xoff, woff, do_touch, touch, dst, dm, dc, du);
    STAMP(2);
    asm volatile("s_waitcnt vmcnt(0)" : "+v"(touch) :: "memory");
    const float sc = (bj < 8) ? QSCALE : 1.f;
#pragma unroll
    for (int m = 0; m < 2; ++m)
#pragma unroll
        for (int c = 0; c < 2; ++c) {
            const f32x16 a = acc[m][c];
            unsigned w[8];
#pragma unroll
            for (int i = 0; i < 8; ++i) w[i] = cvtpk(a[2 * i] * sc, a[2 * i + 1] * sc);
#pragma unroll
            for (int u = 0; u < 2; ++u) {
                auto s0 = __builtin_amdgcn_permlane32_swap(w[4 * u + 0], w[4 * u + 2], false, false);
                auto s1 = __builtin_amdgcn_permlane32_swap(w[4 * u + 1], w[4 * u + 3], false, false);
                ST16A(dst + m * dm + c * dc + u * du, ((u32x4){s0[0], s1[0], s0[1], s1[1]}));
            }
        }
    if (bj >= 12 && (r & 15) < 2) {
        char* vb = dst - h * 256 - (r & 15) * 16 + h * VBLK + 512 + (r & 1) * 16;
        const u32x4 cv = (r & 1) ? (u32x4){0u, 0u, 0u, 0u} : ones;
#pragma unroll
        for (int m = 0; m < 2; ++m)
#pragma unroll
            for (int c = 0; c < 2; ++c) *(u32x4*)(vb + m * dm + c * dc) = cv;
    }
    asm volatile("s_waitcnt vmcnt(0)" : "+v"(touch) :: "memory");
    STAMP(3); STAMP_OUT(0);
}

constexpr int A_RED = (int)KV_HEAD;
constexpr int A_GATE = A_RED + 24576, A_SYNC = A_GATE + 2048, A_LDS = A_SYNC + 64;

#define PIN(x) asm volatile("" : "+v"(x))
#define SMF(acc, vf, p) do { acc = __builtin_amdgcn_smfmac_f32_32x32x32_bf16(vf, __builtin_bit_cast(bf16x16, p), acc, sidx, 0, 0); PIN(acc); } while (0)
#define EXPR(S, lo, hi) _Pragma("unroll") for (int e_ = lo; e_ < hi; ++e_) S[e_] = __builtin_amdgcn_exp2f(S[e_])
#define LO8(S) __builtin_shufflevector(S, S, 0, 1, 2, 3, 4, 5, 6, 7)
#define HI8(S) __builtin_shufflevector(S, S, 8, 9, 10, 11, 12, 13, 14, 15)

__device__ __forceinline__ float max16(const f32x16& s) {
    float a = fmaxf(fmaxf(s[0], s[1]), s[2]), b = fmaxf(fmaxf(s[3], s[4]), s[5]);
    a = fmaxf(fmaxf(a, s[6]), s[7]); b = fmaxf(fmaxf(b, s[8]), s[9]);
    a = fmaxf(fmaxf(a, s[10]), s[11]); b = fmaxf(fmaxf(b, s[12]), s[13]);
    a = fmaxf(fmaxf(a, s[14]), s[15]);
    return fmaxf(a, b);
}
__device__ __forceinline__ float xhalf_max(float m) {
    auto rr = __builtin_amdgcn_permlane32_swap(__float_as_uint(m), __float_as_uint(m), false, false);
    return fmaxf(__uint_as_float(rr[0]), __uint_as_float(rr[1]));
}

#define ATT_STEP(KN, V1C, V0N) do { \
    SBAR(); EXPR(SA, 0, 2);   SBAR(); SMF(OB, v1, HI8(SB)); v1 = ld128(V1C); \
    SBAR(); EXPR(SA, 2, 6);   SBAR(); SB = MFMA32(kf, qbf, CB); PIN(SB); kf = ld128(KN); \
    SBAR(); EXPR(SA, 6, 10);  SBAR(); SMF(OA, v0, LO8(SA)); \
    SBAR(); EXPR(SA, 10, 16); EXPR(SB, 0, 2); SBAR(); SMF(OA, v1, HI8(SA)); \
    SBAR(); EXPR(SB, 2, 6);   SBAR(); SA = MFMA32(kf, qa, CA); PIN(SA); \
    SBAR(); EXPR(SB, 6, 10);  SBAR(); SMF(OB, v0, LO8(SB)); v0 = ld128(V0N); \
    SBAR(); EXPR(SB, 10, 16); } while (0)

#define POLL_SLEEP 4
#define LATE_DELAY 8
#define PRIO_C 5
typedef __attribute__((address_space(1))) unsigned gu32;
typedef __attribute__((address_space(3))) unsigned lu32;
__device__ __forceinline__ unsigned lds_ld(lu32* p) { return __builtin_amdgcn_readfirstlane(__hip_atomic_load(p, __ATOMIC_RELAXED, __HIP_MEMORY_SCOPE_WORKGROUP)); }
__device__ __forceinline__ void lds_arrive_wait(lu32* w, unsigned target, int lane) {
    asm volatile("s_waitcnt lgkmcnt(0)" ::: "memory");
    if (lane == 0) __hip_atomic_fetch_add(w, 1u, __ATOMIC_RELAXED, __HIP_MEMORY_SCOPE_WORKGROUP);
#pragma unroll 1
    for (unsigned spins = 0; spins < (1u << 22); ++spins) { if (lds_ld(w) >= target) break; __builtin_amdgcn_s_sleep(1); }
    asm volatile("" ::: "memory");
}

__global__ __launch_bounds__(512, 2) void k_attn(char* __restrict__ ws, u16* __restrict__ out) {
    extern __shared__ __attribute__((aligned(16))) char smem[];
    const int tid = threadIdx.x, lane = tid & 63, wid = __builtin_amdgcn_readfirstlane(tid >> 6);
    const int bid = blockIdx.x, grp = bid & 7, idx = bid >> 3;
    const int head = (idx >> 4) * 32 + (grp & 1) * 16 + (idx & 15), qb = grp >> 1;
    const int ort = idx & 7, obn = (grp & 1) * 4 + (idx >> 3);
    const int kvh = head < 32 ? 0 : head - 31;
    const unsigned lds0 = (unsigned)(uintptr_t)smem;
    const int q0 = qb * 512 + wid * 64;
    STAMP_DECL; STAMP(0);
    const int r = lane & 31, h = lane >> 5;
    bf16x8 qa, qbf;
    {
        const char* qsrc = ws + WS_Q + (size_t)(head * SEQ + q0) * 32 + lane * 16;
        asm volatile("global_load_dwordx4 %0, %2, off\n\tglobal_load_dwordx4 %1, %2, off offset:1024"
                     : "=&v"(qa), "=&v"(qbf) : "v"(qsrc) : "memory");
    }
    const char* ksrc = ws + WS_KV + (size_t)kvh * KV_HEAD + lane * 16;
#define KVDMA(p) do { int p_ = qb * 33 + (p); p_ = p_ >= 132 ? p_ - 132 : p_; glds16(ksrc + p_ * 1024, lds0 + (p) * 1024); } while (0)
    KVDMA(wid);
    STAMP(14);
    const __amdgpu_buffer_rsrc_t rsO = __builtin_amdgcn_make_buffer_rsrc((void*)(ws + WS_O), 0, 64 * SEQ * DH * 2, 0x00020000);
    const unsigned ooff = (unsigned)(head * SEQ + q0) * 32 + lane * 16;
    const u32x4 poison = {~0u, ~0u, ~0u, ~0u};
    lds_cptr L = (lds_cptr)smem;
    lu32* sync = (lu32*)(smem + A_SYNC);
    if (tid < 16) sync[tid] = 0u;
    lds_cptr kp = L + lane * 16;
    lds_cptr vp = L + 1024 + (r < 16 ? h * 256 + r * 16 : ((r == 16 || r == 20) ? 512 : 528));
    int sidx = 0xDDDD; PIN(sidx);
    f32x16 OA, OB, CA, CB, SA, SB;
#pragma unroll
    for (int i = 0; i < 16; ++i) { OA[i] = 0.f; OB[i] = 0.f; SB[i] = 0.f; }
    asm volatile("" : "+v"(OA), "+v"(OB), "+v"(SB));
    asm volatile("s_waitcnt vmcnt(0) lgkmcnt(0)\n\ts_barrier" : "+v"(qa), "+v"(qbf) :: "memory");
#define LANDED(w) do { if (lane == 0) __hip_atomic_fetch_add(sync + 8 + (w), 1u, __ATOMIC_RELAXED, __HIP_MEMORY_SCOPE_WORKGROUP); } while (0)
#define AWAIT(w) do { SBAR(); _Pragma("unroll 1") for (unsigned sp_ = 0; sp_ < (1u << 22); ++sp_) { if (lds_ld(sync + 8 + (w)) >= 4u) break; __builtin_amdgcn_s_sleep(1); } \
                      asm volatile("" ::: "memory"); SBAR(); } while (0)
    if (wid >= 4) {
#pragma unroll 1
        for (int i = 0; i < 8; ++i) KVDMA(4 + wid + 4 * i);
        asm volatile("s_waitcnt vmcnt(4)" ::: "memory"); LANDED(0);
#pragma unroll 1
        for (int i = 8; i < 16; ++i) KVDMA(4 + wid + 4 * i);
        asm volatile("s_waitcnt vmcnt(8)" ::: "memory"); LANDED(1);
#pragma unroll 1
        for (int i = 16; i < 24; ++i) KVDMA(4 + wid + 4 * i);
        asm volatile("s_waitcnt vmcnt(8)" ::: "memory"); LANDED(2);
#pragma unroll 1
        for (int i = 24; i < 31; ++i) KVDMA(4 + wid + 4 * i);
        if (wid < 6) glds16(ws + WS_G + ((head < 32 ? 0 : SEQ) + qb * 512) * 4 + (wid - 4) * 1024 + lane * 16, lds0 + A_GATE + (wid - 4) * 1024);
        __builtin_amdgcn_raw_buffer_store_b128(poison, rsO, ooff - 8192, 0, 16); __builtin_amdgcn_raw_buffer_store_b128(poison, rsO, ooff - 7168, 0, 16);
        __builtin_amdgcn_raw_buffer_store_b128(poison, rsO, ooff, 0, 16); __builtin_amdgcn_raw_buffer_store_b128(poison, rsO, ooff + 1024, 0, 16);
        asm volatile("s_waitcnt vmcnt(0)" ::: "memory");
        unsigned prev = 0;
        if (lane == 0) prev = __hip_atomic_fetch_add(sync + 8 + 3, 1u, __ATOMIC_RELAXED, __HIP_MEMORY_SCOPE_WORKGROUP);
        if (__builtin_amdgcn_readfirstlane(prev) == 3u && lane < 8)
            __hip_atomic_fetch_add((gu32*)(ws + WS_CNT + (qb * 8 + lane) * 256), 1u, __ATOMIC_RELAXED, __HIP_MEMORY_SCOPE_AGENT);
        AWAIT(3);
        __builtin_amdgcn_s_setprio(1);
    }
    STAMP(1);
    float ma, mb;
    bf16x8 kf, v0, v1;
    {
        const f32x16 z = {};
        kf = ld128(kp);
        const bf16x8 k1 = ld128(kp + KV_TILE), k2 = ld128(kp + 2 * KV_TILE), k3 = ld128(kp + 3 * KV_TILE);
        v0 = ld128(vp); v1 = ld128(vp + VBLK);
        const f32x16 s0 = MFMA32(kf, qa, z), s1 = MFMA32(k1, qa, z), s2 = MFMA32(kf, qbf, z), s3 = MFMA32(k1, qbf, z);
        ma = fmaxf(max16(s0), max16(s1)); mb = fmaxf(max16(s2), max16(s3));
        const f32x16 s4 = MFMA32(k2, qa, z), s5 = MFMA32(k3, qa, z), s6 = MFMA32(k2, qbf, z), s7 = MFMA32(k3, qbf, z);
        ma = xhalf_max(fmaxf(ma, fmaxf(max16(s4), max16(s5))));
        mb = xhalf_max(fmaxf(mb, fmaxf(max16(s6), max16(s7))));
    }
    for (int attempt = 0; attempt < 2; ++attempt) {
#pragma unroll
        for (int i = 0; i < 16; ++i) { CA[i] = -ma; CB[i] = -mb; }
        asm volatile("" : "+v"(CA), "+v"(CB));
        SA = MFMA32(kf, qa, CA);
        if (attempt == 0) STAMP(2);
#pragma unroll 1
        for (int c = 0; c < 8; ++c) {
            if (c == PRIO_C) __builtin_amdgcn_s_setprio(0);
            if (c < 3 || c == 4) AWAIT(c < 3 ? c : 3);
            lds_cptr kc = kp + c * 8 * KV_TILE;
            lds_cptr vc = vp + c * 8 * KV_TILE;
#pragma unroll
            for (int t = 0; t < 8; ++t) {
                ATT_STEP(kc + (t + 1) * KV_TILE, vc + t * KV_TILE + VBLK, vc + (t + 1) * KV_TILE);
            }
        }
        SBAR();
        SMF(OB, v1, HI8(SB));
        if (attempt == 0) STAMP(3); else STAMP(5);
        const float la = OA[8], lb = OB[8];
        float oa = 0.f, ob = 0.f;
#pragma unroll
        for (int i = 0; i < 8; ++i) { oa += fabsf(OA[i]); ob += fabsf(OB[i]); }
        const bool ok = (la <= 0x1p125f) && (lb <= 0x1p125f) && (oa <= 0x1p126f) && (ob <= 0x1p126f);
        if (attempt == 1 || __all(ok)) break;
        const f32x16 z = {};
        float ta = -INFINITY, tb = -INFINITY;
#pragma unroll 1
        for (int t = 0; t < 64; ++t) {
            const bf16x8 k0 = ld128(kp + t * KV_TILE);
            ta = fmaxf(ta, max16(MFMA32(k0, qa, z)));
            tb = fmaxf(tb, max16(MFMA32(k0, qbf, z)));
        }
        ma = xhalf_max(ta); mb = xhalf_max(tb);
#pragma unroll
        for (int i = 0; i < 16; ++i) { OA[i] = 0.f; OB[i] = 0.f; SB[i] = 0.f; }
        kf = ld128(kp); v0 = ld128(vp); v1 = ld128(vp + VBLK);
    }
    {
        const float* gl = (const float*)(smem + A_GATE) + wid * 64 + r;
        const float sa = gl[0] * __builtin_amdgcn_rcpf(OA[8]), sb = gl[32] * __builtin_amdgcn_rcpf(OB[8]);
        unsigned w[4];
#pragma unroll
        for (int i = 0; i < 4; ++i) w[i] = min(cvtpk(OA[2 * i] * sa, OA[2 * i + 1] * sa), 0xfffffffeu);
        auto s0 = __builtin_amdgcn_permlane32_swap(w[0], w[2], false, false);
        auto s1 = __builtin_amdgcn_permlane32_swap(w[1], w[3], false, false);
        __builtin_amdgcn_raw_buffer_store_b128((u32x4){s0[0], s1[0], s0[1], s1[1]}, rsO, ooff, 0, 16);
#pragma unroll
        for (int i = 0; i < 4; ++i) w[i] = min(cvtpk(OB[2 * i] * sb, OB[2 * i + 1] * sb), 0xfffffffeu);
        auto t0 = __builtin_amdgcn_permlane32_swap(w[0], w[2], false, false);
        auto t1 = __builtin_amdgcn_permlane32_swap(w[1], w[3], false, false);
        __builtin_amdgcn_raw_buffer_store_b128((u32x4){t0[0], t1[0], t0[1], t1[1]}, rsO, ooff + 1024, 0, 16);
        if (wid >= 4) {
            if (lane == 0) __hip_atomic_fetch_add(sync + 1, 1u, __ATOMIC_RELAXED, __HIP_MEMORY_SCOPE_WORKGROUP);
            STAMP(4); STAMP_OUT(1);
            return;
        }
    }
    STAMP(4);
    const int bn = obn, kq = wid;
    bf16x8 wf[16][2];
    {
        const int d0 = (kq + (r >> 4)) & 3, d1 = (kq + 2 + (r >> 4)) & 3;
        const char* Wp = ws + WS_WO + ((size_t)(kq * 16) * 16 + bn * 2) * 1024 + (h * 32 + (r & 15)) * 16;
        const char* Wp0 = Wp + (d0 >> 1) * 1024 + (d0 & 1) * 256;
        const char* Wp1 = Wp + (d1 >> 1) * 1024 + (d1 & 1) * 256;
#pragma unroll
        for (int k = 0; k < 16; ++k) { wf[k][0] = *(const bf16x8*)(Wp0 + k * 16384); wf[k][1] = *(const bf16x8*)(Wp1 + k * 16384); }
    }
    {
        gu32* cnt = (gu32*)(ws + WS_CNT + (qb * 8 + ort) * 256);
#pragma unroll 1
        for (unsigned spins = 0; spins < (1u << 20); ++spins) {
            if ((unsigned)__builtin_amdgcn_readfirstlane(__hip_atomic_load(cnt, __ATOMIC_RELAXED, __HIP_MEMORY_SCOPE_AGENT)) >= 64u) break;
            __builtin_amdgcn_s_sleep(8);
        }
        asm volatile("" ::: "memory");
    }
    f32x4* red = (f32x4*)(smem + A_RED);
#pragma unroll 1
    for (int ph = 0; ph < 2; ++ph) {
        const int row0 = qb * 512 + ph * 256 + ort * 32;
        const unsigned aoff = (unsigned)(kq * 16 * SEQ + row0) * 32 + lane * 16;
        if (ph == 1) {
#pragma unroll 1
            for (unsigned spins = 0; spins < (1u << 24); ++spins) { if (lds_ld(sync + 1) >= 4u) break; __builtin_amdgcn_s_sleep(4); }
            __builtin_amdgcn_s_sleep(LATE_DELAY);
        }
        if (ph == 0) STAMP(7); else STAMP(10);
        f32x16 acc[2];
#pragma unroll 1
        for (unsigned spins = 0; spins < (1u << 20); ++spins) {
            asm volatile("" ::: "memory");
            u32x4 of[16];
#pragma unroll
            for (int k = 0; k < 16; ++k) of[k] = __builtin_amdgcn_raw_buffer_load_b128(rsO, aoff, k * SEQ * 32, 16);
            SBAR();
            acc[0] = (f32x16){}; acc[1] = (f32x16){};
            unsigned mx = 0;
#pragma unroll
            for (int k = 0; k < 16; ++k) {
                acc[0] = MFMA32(wf[k][0], __builtin_bit_cast(bf16x8, of[k]), acc[0]);
                acc[1] = MFMA32(wf[k][1], __builtin_bit_cast(bf16x8, of[k]), acc[1]);
                mx = max(max(mx, max(of[k][0], of[k][1])), max(of[k][2], of[k][3]));
                PIN(mx); SBAR();
            }
            if (!__any(mx == ~0u)) break;
            __builtin_amdgcn_s_sleep(POLL_SLEEP);
        }
        if (ph == 0) STAMP(8); else STAMP(11);
        f32x4 own[2];
#pragma unroll
        for (int j = 0; j < 4; ++j)
#pragma unroll
            for (int g = 0; g < 2; ++g) {
                const int e = 4 * (2 * (j & 1) + g);
                const f32x4 v = {acc[j >> 1][e], acc[j >> 1][e + 1], acc[j >> 1][e + 2], acc[j >> 1][e + 3]};
                if (j == 0) own[g] = v;
                else red[((((kq + j) & 3) * 3 + j - 1) * 2 + g) * 64 + lane] = v;
            }
        if (ph == 0) lds_arrive_wait(sync + 4, 4u, lane);
        else LBAR();
#pragma unroll
        for (int sl = 0; sl < 3; ++sl)
#pragma unroll
            for (int g = 0; g < 2; ++g) own[g] += red[((kq * 3 + sl) * 2 + g) * 64 + lane];
        auto s0 = __builtin_amdgcn_permlane32_swap(cvtpk(own[0][0], own[0][1]), cvtpk(own[1][0], own[1][1]), false, false);
        auto s1 = __builtin_amdgcn_permlane32_swap(cvtpk(own[0][2], own[0][3]), cvtpk(own[1][2], own[1][3]), false, false);
        ST16B(out + (size_t)(row0 + r) * DM + bn * 64 + kq * 16 + h * 8, ((u32x4){s0[0], s1[0], s0[1], s1[1]}));
        if (ph == 0) { lds_arrive_wait(sync + 5, 4u, lane); STAMP(9); }
    }
    STAMP(12); STAMP_OUT(1);
}

extern "C" void kernel_launch(void* const* d_in, const int* in_sizes, int n_in,
                              void* d_out, int out_size, void* d_ws, size_t ws_size,
                              hipStream_t stream) {
    const u16* x = (const u16*)d_in[0];
    const u16* wq = (const u16*)d_in[1];
    const u16* wk = (const u16*)d_in[2];
    const u16* wv = (const u16*)d_in[3];
    const u16* woA = (const u16*)d_in[4];
    const u16* wqkv = (const u16*)d_in[6];
    const u16* woB = (const u16*)d_in[7];
    const u16* gw = (const u16*)d_in[8];
    char* ws = (char*)d_ws;
    (void)hipFuncSetAttribute((const void*)k_proj, hipFuncAttributeMaxDynamicSharedMemorySize, P_LDS);
    (void)hipFuncSetAttribute((const void*)k_attn, hipFuncAttributeMaxDynamicSharedMemorySize, A_LDS);
    k_proj<<<256, 640, P_LDS, stream>>>(x, wq, wk, wv, wqkv, woA, woB, gw, ws);
    k_attn<<<256, 512, A_LDS, stream>>>(ws, (u16*)d_out);
}
```

```cpp
#include <hip/hip_runtime.h>
#include <hip/hip_bf16.h>
#include <stdint.h>

typedef __attribute__((ext_vector_type(8))) __bf16 bf16x8;
typedef __attribute__((ext_vector_type(16))) __bf16 bf16x16;
typedef __attribute__((ext_vector_type(4))) short s16x4;
typedef __attribute__((ext_vector_type(8))) short s16x8;
typedef __attribute__((ext_vector_type(4))) float f32x4;
typedef __attribute__((ext_vector_type(8))) float f32x8;
typedef __attribute__((ext_vector_type(16))) float f32x16;
typedef __attribute__((ext_vector_type(4))) unsigned u32x4;
typedef __attribute__((ext_vector_type(2))) unsigned u32x2;
typedef unsigned short u16;
typedef __attribute__((address_space(3))) const char* lds_cptr;
typedef __attribute__((address_space(3))) char* lds_ptr;

constexpr int SEQ = 2048, DM = 512, NH = 32, DH = 16;
constexpr float QSCALE = 0.25f * 1.4426950408889634f;

constexpr int VBLK = 544;
constexpr size_t KV_TILE = 1024 + 2 * VBLK;
constexpr size_t KV_HEAD = 64 * KV_TILE;
constexpr size_t WS_Q = 0;
constexpr size_t WS_O = WS_Q + (size_t)64 * SEQ * DH * 2;
constexpr size_t WS_WO = WS_O + (size_t)64 * SEQ * DH * 2;
constexpr size_t WS_G = WS_WO + (size_t)1024 * 512 * 2;
constexpr size_t WS_CNT = WS_G + 2 * SEQ * 4;
constexpr size_t WS_KV = WS_CNT + 64 * 256;
constexpr size_t WS_END = WS_KV + 34 * KV_HEAD;

#define SBAR() __builtin_amdgcn_sched_barrier(0)
#define MFMA32(a, b, c) __builtin_amdgcn_mfma_f32_32x32x16_bf16(a, b, c, 0, 0, 0)
#define MFMA16(a, b, c) __builtin_amdgcn_mfma_f32_16x16x32_bf16(a, b, c, 0, 0, 0)

typedef __attribute__((ext_vector_type(2))) __bf16 bf16x2;
typedef __attribute__((ext_vector_type(2))) float f32x2;
__device__ __forceinline__ unsigned cvtpk(float lo, float hi) {
    return __builtin_bit_cast(unsigned, __builtin_convertvector((f32x2){lo, hi}, bf16x2));
}
__device__ __forceinline__ float bf2f(u16 v) { return __uint_as_float((unsigned)v << 16); }
__device__ __forceinline__ float rbf(float v) { return __uint_as_float(cvtpk(v, 0.f) << 16); }

__device__ __forceinline__ void glds16(const void* g, unsigned lds_base) {
    unsigned sv;
    asm volatile("s_mov_b32 %0, m0\n\ts_mov_b32 m0, %2\n\ts_nop 0\n\tglobal_load_lds_dwordx4 %1, off\n\ts_mov_b32 m0, %0"
                 : "=&s"(sv) : "v"(g), "s"(lds_base) : "memory");
}
__device__ __forceinline__ void touch4(const void* g, unsigned lds_dummy) {
    unsigned sv;
    asm volatile("s_mov_b32 %0, m0\n\ts_mov_b32 m0, %2\n\ts_nop 0\n\tglobal_load_lds_dword %1, off\n\ts_mov_b32 m0, %0"
                 : "=&s"(sv) : "v"(g), "s"(lds_dummy) : "memory");
}
__device__ __forceinline__ s16x4 ldtr(lds_cptr p) {
    typedef short v4i16_t __attribute__((ext_vector_type(4)));
    return __builtin_bit_cast(s16x4, __builtin_amdgcn_ds_read_tr16_b64_v4i16((__attribute__((address_space(3))) v4i16_t*)p));
}
__device__ __forceinline__ bf16x8 ld128(lds_cptr p) { return *(const __attribute__((address_space(3))) bf16x8*)p; }
__device__ __forceinline__ bf16x8 cat8(s16x4 a, s16x4 b) {
    s16x8 v = {a[0], a[1], a[2], a[3], b[0], b[1], b[2], b[3]}; return __builtin_bit_cast(bf16x8, v);
}
#define WAITV_BAR(N) asm volatile("s_waitcnt vmcnt(" #N ") lgkmcnt(0)\n\ts_barrier" ::: "memory")

constexpr int P_STAGE = 32768, P_NST = 4, P_EXTRA = P_NST * P_STAGE, P_GW = P_EXTRA + 16384, P_WO = P_GW + 1024, P_DUMMY = P_WO + 4096, P_LDS = P_DUMMY + 256;

__device__ __forceinline__ unsigned wswz(unsigned row) { return ((row & 3) << 2) | ((row >> 2) & 3); }
#define LBAR() asm volatile("s_waitcnt lgkmcnt(0)\n\ts_barrier" ::: "memory")

#define PROJ_CODE_LINES 56
#define ATTN_CODE_LINES 64
constexpr int TOUCH_KC = 2;
template <bool VT>
__device__ __forceinline__ void proj_loop(f32x16 (&acc)[2][2], lds_cptr L, const unsigned (&xoff)[2][4], const unsigned (&woff)[2][2],
                                          bool do_touch, unsigned dummy, const char* dst, int dm, int dc, int du) {
    bf16x8 xf[2][2], wf[2][2];
#define PMM(m, c, buf) acc[m][c] = VT ? MFMA32(xf[buf][m], wf[buf][c], acc[m][c]) : MFMA32(wf[buf][c], xf[buf][m], acc[m][c])
#define PRD(S, s, buf, i) do { xf[buf][i] = ld128(S + xoff[i][s]); wf[buf][i] = cat8(ldtr(S + woff[i][0] + (s) * 4096), ldtr(S + woff[i][1] + (s) * 4096)); } while (0)
    LBAR();
    PRD(L, 0, 0, 0); PRD(L, 0, 0, 1);
#pragma unroll 1
    for (int kc = 0; kc < 8; ++kc) {
        lds_cptr S = L + (kc & 3) * P_STAGE;
        lds_cptr SN = L + ((kc + 1) & 3) * P_STAGE;
        if (kc == TOUCH_KC && do_touch) {
#pragma unroll
            for (int i = 0; i < 8; ++i)
                touch4(dst + (i >> 2) * dm + ((i >> 1) & 1) * dc + (i & 1) * du, dummy);
        }
#pragma unroll
        for (int s = 0; s < 4; ++s) {
            SBAR();
            if (s == 3 && kc < 7) LBAR();
            PMM(0, 0, s & 1);
            SBAR();
            if (s < 3) PRD(S, s + 1, (s + 1) & 1, 0); else PRD(SN, 0, 0, 0);
            SBAR();
            PMM(0, 1, s & 1);
            SBAR();
            if (s < 3) PRD(S, s + 1, (s + 1) & 1, 1); else PRD(SN, 0, 0, 1);
            SBAR();
            PMM(1, 0, s & 1);
            PMM(1, 1, s & 1);
        }
    }
#undef PMM
#undef PRD
}

__global__ __launch_bounds__(640) void k_proj(const u16* __restrict__ x, const u16* __restrict__ wq, const u16* __restrict__ wk,
                                                 const u16* __restrict__ wv, const u16* __restrict__ wqkv, const u16* __restrict__ woA,
                                                 const u16* __restrict__ woB, const u16* __restrict__ gw, char* __restrict__ ws) {
    extern __shared__ __attribute__((aligned(16))) char smem[];
    const int tid = threadIdx.x, lane = tid & 63, wid = __builtin_amdgcn_readfirstlane(tid >> 6);
    const int bid = blockIdx.x, grp = bid & 7, idx = bid >> 3;
    if (wid >= 8 && idx == 0) {
        unsigned long long pc; int rel0, rel1;
        asm volatile("s_getpc_b64 %0\n\ts_mov_b32 %1, _Z6k_projPKtS0_S0_S0_S0_S0_S0_S0_Pc@rel32@lo+4\n\ts_mov_b32 %2, _Z6k_attnPcPt@rel32@lo+12"
                     : "=s"(pc), "=s"(rel0), "=s"(rel1));
        const char* cp = (const char*)pc + (wid == 8 ? rel0 : rel1) + lane * 128;
        if (lane < (wid == 8 ? PROJ_CODE_LINES : ATTN_CODE_LINES))
            touch4(cp, (unsigned)(uintptr_t)smem + P_DUMMY);
    }
    asm volatile("" :: "s"(x), "s"(wq), "s"(wqkv), "s"(gw), "s"(ws));
    const int cj = idx >> 2;
    const int bi = (grp >> 1) * 4 + (idx & 3);
    const int bj = (cj >> 1) * 4 + (grp & 1) * 2 + (cj & 1);
    const int rot = grp;
    const unsigned lds0 = (unsigned)(uintptr_t)smem;
    lds_cptr L = (lds_cptr)smem;
    char* kvbase = ws + WS_KV;
    const u32x4 ones = {0x3f803f80u, 0x3f803f80u, 0x3f803f80u, 0x3f803f80u};
    if (__builtin_expect((wid & 12) == 4, 1)) {
        const int lw = wid - 4;
        const u16* W; int ldw, col0;
        if (bj < 4) { W = wq; ldw = 512; col0 = bj * 128; } else { W = wqkv; ldw = 1536; col0 = (bj - 4) * 128; }
        const u16* xs; const u16* wsrc;
        {
            const int row = 8 * lw + (lane >> 3), ch = (lane & 7) ^ ((row >> 1) & 7);
            xs = x + (size_t)(bi * 128 + row) * DM + ch * 8;
            const int kr = 4 * lw + (lane >> 4), wch = (lane & 15) ^ wswz(kr);
            wsrc = W + (size_t)kr * ldw + col0 + wch * 8;
        }
#define PSTAGE(s) do { const unsigned base_ = lds0 + ((s) & (P_NST - 1)) * P_STAGE + lw * 1024; const int kc_ = ((s) + rot) & 7; \
        _Pragma("unroll") for (int i = 0; i < 4; ++i) { glds16(xs + kc_ * 64 + i * 32 * DM, base_ + i * 4096); glds16(wsrc + (kc_ * 64 + i * 16) * ldw, base_ + 16384 + i * 4096); } } while (0)
        PSTAGE(0);
        PSTAGE(1);
        {
            const u16* we = (bj & 1) ? wv : wk;
#pragma unroll
            for (int i = 0; i < 4; ++i) glds16(we + (lw + 4 * i) * 512 + lane * 8, lds0 + P_EXTRA + (lw + 4 * i) * 1024);
            glds16(gw + lane * 8, lds0 + P_GW);
        }
        WAITV_BAR(13);
        PSTAGE(2);
#pragma unroll 1
        for (int kc = 1; kc < 6; ++kc) {
            WAITV_BAR(8);
            PSTAGE(kc + 2);
        }
        {
            const int ks = bid >> 2, kr = 4 * lw + (lane >> 4);
            const u16* wo = (ks < 32) ? woA + (size_t)ks * 16 * DM : woB + (size_t)(ks - 32) * 16 * DM;
            glds16(wo + (size_t)kr * DM + (bid & 3) * 128 + ((lane & 15) ^ wswz(kr)) * 8, lds0 + P_WO + lw * 1024);
        }
        WAITV_BAR(9);
        WAITV_BAR(0);
        {
            const int h = lane >> 5, blk = (lane >> 4) & 1, q = (lane & 15) >> 2, p = lane & 3;
            const int kr0 = 8 * h + q, kr1 = kr0 + 4, ch = 4 * lw + 2 * blk + (p >> 1);
            lds_cptr wb = L + P_WO + 8 * (p & 1);
            *(bf16x8*)(ws + WS_WO + ((size_t)((bid >> 2) * 16 + (bid & 3) * 4 + lw) * 64 + lane) * 16) =
                cat8(ldtr(wb + 256 * kr0 + 16 * (ch ^ wswz(kr0))), ldtr(wb + 256 * kr1 + 16 * (ch ^ wswz(kr1))));
        }
        return;
    }
    if (wid >= 8) {
        const int erow = (bj >> 1) * 16 + (lane & 15), eg = lane >> 4;
        const unsigned ex0 = erow * 128 + 16 * (eg ^ ((erow >> 1) & 7)), ex1 = erow * 128 + 16 * ((4 + eg) ^ ((erow >> 1) & 7));
        lds_cptr ewp = L + P_EXTRA + (8 * eg + ((lane & 15) >> 2)) * 32 + 8 * (lane & 3);
        lds_cptr gwp = L + P_GW + eg * 16;
        f32x4 eacc = {};
        __builtin_amdgcn_s_setprio(3);
        LBAR();
#pragma unroll 1
        for (int kc = 0; kc < 8; ++kc) {
            if (kc < 7) LBAR();
            lds_cptr S = L + (kc & 3) * P_STAGE;
            const int kg = (kc + rot) & 7;
            if (wid == 8) {
                eacc = MFMA16(ld128(S + ex0), cat8(ldtr(ewp + kg * 2048), ldtr(ewp + kg * 2048 + 128)), eacc);
                eacc = MFMA16(ld128(S + ex1), cat8(ldtr(ewp + kg * 2048 + 1024), ldtr(ewp + kg * 2048 + 1152)), eacc);
            } else {
                eacc = MFMA16(ld128(S + ex0), ld128(gwp + kg * 128), eacc);
                eacc = MFMA16(ld128(S + ex1), ld128(gwp + kg * 128 + 64), eacc);
            }
        }
        const int key0 = bi * 128 + (bj >> 1) * 16;
        if (wid == 8) {
            const unsigned e0 = cvtpk(eacc[0], eacc[1]), e1 = cvtpk(eacc[2], eacc[3]);
            char* tb = kvbase + (size_t)(key0 >> 5) * KV_TILE;
            const int u = (key0 >> 4) & 1, d = lane & 15;
            if (bj & 1) {
                *(u32x2*)(tb + 1024 + u * VBLK + (eg >> 1) * 256 + d * 16 + (eg & 1) * 8) = (u32x2){e0, e1};
                if (lane < 2) *(u32x4*)(tb + 1024 + u * VBLK + 512 + lane * 16) = lane ? (u32x4){0u, 0u, 0u, 0u} : ones;
            } else {
                u16* kb = (u16*)(tb + (d >> 3) * 512 + (u * 16 + 4 * eg) * 16 + (d & 7) * 2);
                kb[0] = (u16)e0; kb[8] = (u16)(e0 >> 16); kb[16] = (u16)e1; kb[24] = (u16)(e1 >> 16);
            }
        } else {
            if (!(bj & 1) && (lane & 15) == 0) {
                f32x4 g, og;
#pragma unroll
                for (int i = 0; i < 4; ++i) { g[i] = rbf(__builtin_amdgcn_rcpf(1.f + __expf(-rbf(eacc[i])))); og[i] = rbf(1.f - g[i]); }
                float* G = (float*)(ws + WS_G);
                *(f32x4*)(G + key0 + 4 * eg) = g; *(f32x4*)(G + SEQ + key0 + 4 * eg) = og;
            }
            if (bid == 0) *(unsigned*)(ws + WS_CNT + lane * 256) = 0u;
        }
        return;
    }
    const int wr = wid >> 1, wc = wid & 1, r = lane & 31, h = lane >> 5;
    f32x16 acc[2][2] = {};
    unsigned xoff[2][4], woff[2][2];
#pragma unroll
    for (int m = 0; m < 2; ++m)
#pragma unroll
        for (int s = 0; s < 4; ++s) { const int row = wr * 64 + m * 32 + r; xoff[m][s] = row * 128 + 16 * ((2 * s + h) ^ ((row >> 1) & 7)); }
    {
        const int blk = (lane >> 4) & 1, q = (lane & 15) >> 2, p = lane & 3;
#pragma unroll
        for (int t = 0; t < 2; ++t) {
            const int krow = 8 * h + 4 * t + q;
#pragma unroll
            for (int c = 0; c < 2; ++c)
                woff[c][t] = 16384 + 256 * krow + 16 * ((4 * (wc * 2 + c) + 2 * blk + (p >> 1)) ^ wswz(krow)) + 8 * (p & 1);
        }
    }
    char* dst; int dm, dc, du;
    {
        const int srow0 = bi * 128 + wr * 64, hd0 = (bj & 3) * 8 + wc * 4;
        if (bj < 8) {
            dst = ws + WS_Q + (size_t)(((bj >> 2) * 32 + hd0) * SEQ + srow0) * 32 + lane * 16;
            dm = 32 * 32; du = SEQ * 32; dc = 2 * du;
        } else if (bj < 12) {
            dst = kvbase + (size_t)(1 + hd0) * KV_HEAD + (srow0 >> 5) * KV_TILE + h * 512 + r * 16;
            dm = (int)KV_TILE; du = (int)KV_HEAD; dc = 2 * du;
        } else {
            dst = kvbase + (size_t)(1 + hd0 + (r >> 4)) * KV_HEAD + (srow0 >> 5) * KV_TILE + 1024 + h * 256 + (r & 15) * 16;
            dm = (int)KV_TILE; du = VBLK; dc = 2 * (int)KV_HEAD;
        }
    }
    const bool do_touch = (bj < 8) ? (lane & 7) == 0 : ((idx & 3) == 0 && (lane & 3) == 0);
    if ((lane & 47) == 0) {
#pragma unroll
        for (int i = 0; i < 8; ++i)
            touch4(dst + (i >> 2) * dm + ((i >> 1) & 1) * dc + (i & 1) * du, lds0 + P_DUMMY);
    }
    if (bj >= 12) proj_loop<true>(acc, L, xoff, woff, do_touch, lds0 + P_DUMMY, dst, dm, dc, du);
    else proj_loop<false>(acc, L, xoff, woff, do_touch, lds0 + P_DUMMY, dst, dm, dc, du);
    asm volatile("s_waitcnt vmcnt(0)" ::: "memory");
    const float sc = (bj < 8) ? QSCALE : 1.f;
#pragma unroll
    for (int m = 0; m < 2; ++m)
#pragma unroll
        for (int c = 0; c < 2; ++c) {
            const f32x16 a = acc[m][c];
            unsigned w[8];
#pragma unroll
            for (int i = 0; i < 8; ++i) w[i] = cvtpk(a[2 * i] * sc, a[2 * i + 1] * sc);
#pragma unroll
            for (int u = 0; u < 2; ++u) {
                auto s0 = __builtin_amdgcn_permlane32_swap(w[4 * u + 0], w[4 * u + 2], false, false);
                auto s1 = __builtin_amdgcn_permlane32_swap(w[4 * u + 1], w[4 * u + 3], false, false);
                __builtin_nontemporal_store((u32x4){s0[0], s1[0], s0[1], s1[1]}, (u32x4*)(dst + m * dm + c * dc + u * du));
            }
        }
    if (bj >= 12 && (r & 15) < 2) {
        char* vb = dst - h * 256 - (r & 15) * 16 + h * VBLK + 512 + (r & 1) * 16;
        const u32x4 cv = (r & 1) ? (u32x4){0u, 0u, 0u, 0u} : ones;
#pragma unroll
        for (int m = 0; m < 2; ++m)
#pragma unroll
            for (int c = 0; c < 2; ++c) *(u32x4*)(vb + m * dm + c * dc) = cv;
    }
}

constexpr int A_RED = (int)KV_HEAD;
constexpr int A_GATE = A_RED + 24576, A_SYNC = A_GATE + 2048, A_LDS = A_SYNC + 64;

#define PIN(x) asm volatile("" : "+v"(x))
#define SMF(acc, vf, p) do { acc = __builtin_amdgcn_smfmac_f32_32x32x32_bf16(vf, __builtin_bit_cast(bf16x16, p), acc, sidx, 0, 0); PIN(acc); } while (0)
#define EXPR(S, lo, hi) _Pragma("unroll") for (int e_ = lo; e_ < hi; ++e_) S[e_] = __builtin_amdgcn_exp2f(S[e_])
#define LO8(S) __builtin_shufflevector(S, S, 0, 1, 2, 3, 4, 5, 6, 7)
#define HI8(S) __builtin_shufflevector(S, S, 8, 9, 10, 11, 12, 13, 14, 15)

__device__ __forceinline__ float max16(const f32x16& s) {
    float a = fmaxf(fmaxf(s[0], s[1]), s[2]), b = fmaxf(fmaxf(s[3], s[4]), s[5]);
    a = fmaxf(fmaxf(a, s[6]), s[7]); b = fmaxf(fmaxf(b, s[8]), s[9]);
    a = fmaxf(fmaxf(a, s[10]), s[11]); b = fmaxf(fmaxf(b, s[12]), s[13]);
    a = fmaxf(fmaxf(a, s[14]), s[15]);
    return fmaxf(a, b);
}
__device__ __forceinline__ float xhalf_max(float m) {
    auto rr = __builtin_amdgcn_permlane32_swap(__float_as_uint(m), __float_as_uint(m), false, false);
    return fmaxf(__uint_as_float(rr[0]), __uint_as_float(rr[1]));
}

#define ATT_STEP(KN, V1C, V0N) do { \
    SBAR(); EXPR(SA, 0, 2);   SBAR(); SMF(OB, v1, HI8(SB)); v1 = ld128(V1C); \
    SBAR(); EXPR(SA, 2, 6);   SBAR(); SB = MFMA32(kf, qbf, CB); PIN(SB); kf = ld128(KN); \
    SBAR(); EXPR(SA, 6, 10);  SBAR(); SMF(OA, v0, LO8(SA)); \
    SBAR(); EXPR(SA, 10, 16); EXPR(SB, 0, 2); SBAR(); SMF(OA, v1, HI8(SA)); \
    SBAR(); EXPR(SB, 2, 6);   SBAR(); SA = MFMA32(kf, qa, CA); PIN(SA); \
    SBAR(); EXPR(SB, 6, 10);  SBAR(); SMF(OB, v0, LO8(SB)); v0 = ld128(V0N); \
    SBAR(); EXPR(SB, 10, 16); } while (0)

constexpr int POLL_SLEEP = 4, LATE_DELAY = 8, PRIO_C = 5;
typedef __attribute__((address_space(1))) unsigned gu32;
typedef __attribute__((address_space(3))) unsigned lu32;
__device__ __forceinline__ unsigned lds_ld(lu32* p) { return __builtin_amdgcn_readfirstlane(__hip_atomic_load(p, __ATOMIC_RELAXED, __HIP_MEMORY_SCOPE_WORKGROUP)); }
__device__ __forceinline__ void lds_arrive_wait(lu32* w, unsigned target, int lane) {
    asm volatile("s_waitcnt lgkmcnt(0)" ::: "memory");
    if (lane == 0) __hip_atomic_fetch_add(w, 1u, __ATOMIC_RELAXED, __HIP_MEMORY_SCOPE_WORKGROUP);
#pragma unroll 1
    for (unsigned spins = 0; spins < (1u << 22); ++spins) { if (lds_ld(w) >= target) break; __builtin_amdgcn_s_sleep(1); }
    asm volatile("" ::: "memory");
}

__global__ __launch_bounds__(512, 2) void k_attn(char* __restrict__ ws, u16* __restrict__ out) {
    extern __shared__ __attribute__((aligned(16))) char smem[];
    const int tid = threadIdx.x, lane = tid & 63, wid = __builtin_amdgcn_readfirstlane(tid >> 6);
    const int bid = blockIdx.x, grp = bid & 7, idx = bid >> 3;
    const int head = (idx >> 4) * 32 + (grp & 1) * 16 + (idx & 15), qb = grp >> 1;
    const int ort = idx & 7, obn = (grp & 1) * 4 + (idx >> 3);
    const int kvh = head < 32 ? 0 : head - 31;
    const unsigned lds0 = (unsigned)(uintptr_t)smem;
    const int q0 = qb * 512 + wid * 64;
    const int r = lane & 31, h = lane >> 5;
    bf16x8 qa, qbf;
    {
        const char* qsrc = ws + WS_Q + (size_t)(head * SEQ + q0) * 32 + lane * 16;
        asm volatile("global_load_dwordx4 %0, %2, off\n\tglobal_load_dwordx4 %1, %2, off offset:1024"
                     : "=&v"(qa), "=&v"(qbf) : "v"(qsrc) : "memory");
    }
    const char* ksrc = ws + WS_KV + (size_t)kvh * KV_HEAD + lane * 16;
#define KVDMA(p) do { int p_ = qb * 33 + (p); p_ = p_ >= 132 ? p_ - 132 : p_; glds16(ksrc + p_ * 1024, lds0 + (p) * 1024); } while (0)
    KVDMA(wid);
    const __amdgpu_buffer_rsrc_t rsO = __builtin_amdgcn_make_buffer_rsrc((void*)(ws + WS_O), 0, 64 * SEQ * DH * 2, 0x00020000);
    const unsigned ooff = (unsigned)(head * SEQ + q0) * 32 + lane * 16;
    const u32x4 poison = {~0u, ~0u, ~0u, ~0u};
    lds_cptr L = (lds_cptr)smem;
    lu32* sync = (lu32*)(smem + A_SYNC);
    if (tid < 16) sync[tid] = 0u;
    lds_cptr kp = L + lane * 16;
    lds_cptr vp = L + 1024 + (r < 16 ? h * 256 + r * 16 : ((r == 16 || r == 20) ? 512 : 528));
    int sidx = 0xDDDD; PIN(sidx);
    f32x16 OA, OB, CA, CB, SA, SB;
#pragma unroll
    for (int i = 0; i < 16; ++i) { OA[i] = 0.f; OB[i] = 0.f; SB[i] = 0.f; }
    asm volatile("" : "+v"(OA), "+v"(OB), "+v"(SB));
    asm volatile("s_waitcnt vmcnt(0) lgkmcnt(0)\n\ts_barrier" : "+v"(qa), "+v"(qbf) :: "memory");
#define LANDED(w) do { if (lane == 0) __hip_atomic_fetch_add(sync + 8 + (w), 1u, __ATOMIC_RELAXED, __HIP_MEMORY_SCOPE_WORKGROUP); } while (0)
#define AWAIT(w) do { SBAR(); _Pragma("unroll 1") for (unsigned sp_ = 0; sp_ < (1u << 22); ++sp_) { if (lds_ld(sync + 8 + (w)) >= 4u) break; __builtin_amdgcn_s_sleep(1); } \
                      asm volatile("" ::: "memory"); SBAR(); } while (0)
    if (wid >= 4) {
#pragma unroll 1
        for (int i = 0; i < 8; ++i) KVDMA(4 + wid + 4 * i);
        asm volatile("s_waitcnt vmcnt(4)" ::: "memory"); LANDED(0);
#pragma unroll 1
        for (int i = 8; i < 16; ++i) KVDMA(4 + wid + 4 * i);
        asm volatile("s_waitcnt vmcnt(8)" ::: "memory"); LANDED(1);
#pragma unroll 1
        for (int i = 16; i < 24; ++i) KVDMA(4 + wid + 4 * i);
        asm volatile("s_waitcnt vmcnt(8)" ::: "memory"); LANDED(2);
#pragma unroll 1
        for (int i = 24; i < 31; ++i) KVDMA(4 + wid + 4 * i);
        if (wid < 6) glds16(ws + WS_G + ((head < 32 ? 0 : SEQ) + qb * 512) * 4 + (wid - 4) * 1024 + lane * 16, lds0 + A_GATE + (wid - 4) * 1024);
        __builtin_amdgcn_raw_buffer_store_b128(poison, rsO, ooff - 8192, 0, 16); __builtin_amdgcn_raw_buffer_store_b128(poison, rsO, ooff - 7168, 0, 16);
        __builtin_amdgcn_raw_buffer_store_b128(poison, rsO, ooff, 0, 16); __builtin_amdgcn_raw_buffer_store_b128(poison, rsO, ooff + 1024, 0, 16);
        asm volatile("s_waitcnt vmcnt(0)" ::: "memory");
        unsigned prev = 0;
        if (lane == 0) prev = __hip_atomic_fetch_add(sync + 8 + 3, 1u, __ATOMIC_RELAXED, __HIP_MEMORY_SCOPE_WORKGROUP);
        if (__builtin_amdgcn_readfirstlane(prev) == 3u && lane < 8)
            __hip_atomic_fetch_add((gu32*)(ws + WS_CNT + (qb * 8 + lane) * 256), 1u, __ATOMIC_RELAXED, __HIP_MEMORY_SCOPE_AGENT);
        AWAIT(3);
        __builtin_amdgcn_s_setprio(1);
    }
    float ma, mb;
    bf16x8 kf, v0, v1;
    {
        const f32x16 z = {};
        kf = ld128(kp);
        const bf16x8 k1 = ld128(kp + KV_TILE), k2 = ld128(kp + 2 * KV_TILE), k3 = ld128(kp + 3 * KV_TILE);
        v0 = ld128(vp); v1 = ld128(vp + VBLK);
        const f32x16 s0 = MFMA32(kf, qa, z), s1 = MFMA32(k1, qa, z), s2 = MFMA32(kf, qbf, z), s3 = MFMA32(k1, qbf, z);
        ma = fmaxf(max16(s0), max16(s1)); mb = fmaxf(max16(s2), max16(s3));
        const f32x16 s4 = MFMA32(k2, qa, z), s5 = MFMA32(k3, qa, z), s6 = MFMA32(k2, qbf, z), s7 = MFMA32(k3, qbf, z);
        ma = xhalf_max(fmaxf(ma, fmaxf(max16(s4), max16(s5))));
        mb = xhalf_max(fmaxf(mb, fmaxf(max16(s6), max16(s7))));
    }
    for (int attempt = 0; attempt < 2; ++attempt) {
#pragma unroll
        for (int i = 0; i < 16; ++i) { CA[i] = -ma; CB[i] = -mb; }
        asm volatile("" : "+v"(CA), "+v"(CB));
        SA = MFMA32(kf, qa, CA);
#pragma unroll 1
        for (int c = 0; c < 8; ++c) {
            if (c == PRIO_C) __builtin_amdgcn_s_setprio(0);
            if (c < 3 || c == 4) AWAIT(c < 3 ? c : 3);
            lds_cptr kc = kp + c * 8 * KV_TILE;
            lds_cptr vc = vp + c * 8 * KV_TILE;
#pragma unroll
            for (int t = 0; t < 8; ++t) {
                ATT_STEP(kc + (t + 1) * KV_TILE, vc + t * KV_TILE + VBLK, vc + (t + 1) * KV_TILE);
            }
        }
        SBAR();
        SMF(OB, v1, HI8(SB));
        const float la = OA[8], lb = OB[8];
        float oa = 0.f, ob = 0.f;
#pragma unroll
        for (int i = 0; i < 8; ++i) { oa += fabsf(OA[i]); ob += fabsf(OB[i]); }
        const bool ok = (la <= 0x1p125f) && (lb <= 0x1p125f) && (oa <= 0x1p126f) && (ob <= 0x1p126f);
        if (attempt == 1 || __all(ok)) break;
        const f32x16 z = {};
        float ta = -INFINITY, tb = -INFINITY;
#pragma unroll 1
        for (int t = 0; t < 64; ++t) {
            const bf16x8 k0 = ld128(kp + t * KV_TILE);
            ta = fmaxf(ta, max16(MFMA32(k0, qa, z)));
            tb = fmaxf(tb, max16(MFMA32(k0, qbf, z)));
        }
        ma = xhalf_max(ta); mb = xhalf_max(tb);
#pragma unroll
        for (int i = 0; i < 16; ++i) { OA[i] = 0.f; OB[i] = 0.f; SB[i] = 0.f; }
        kf = ld128(kp); v0 = ld128(vp); v1 = ld128(vp + VBLK);
    }
    {
        const float* gl = (const float*)(smem + A_GATE) + wid * 64 + r;
        const float ga = gl[0], gb = gl[32], ra = __builtin_amdgcn_rcpf(OA[8]), rb = __builtin_amdgcn_rcpf(OB[8]);
        unsigned w[4];
#pragma unroll
        for (int i = 0; i < 4; ++i) w[i] = min(cvtpk((OA[2 * i] * ra) * ga, (OA[2 * i + 1] * ra) * ga), 0xfffffffeu);
        auto s0 = __builtin_amdgcn_permlane32_swap(w[0], w[2], false, false);
        auto s1 = __builtin_amdgcn_permlane32_swap(w[1], w[3], false, false);
        __builtin_amdgcn_raw_buffer_store_b128((u32x4){s0[0], s1[0], s0[1], s1[1]}, rsO, ooff, 0, 16);
#pragma unroll
        for (int i = 0; i < 4; ++i) w[i] = min(cvtpk((OB[2 * i] * rb) * gb, (OB[2 * i + 1] * rb) * gb), 0xfffffffeu);
        auto t0 = __builtin_amdgcn_permlane32_swap(w[0], w[2], false, false);
        auto t1 = __builtin_amdgcn_permlane32_swap(w[1], w[3], false, false);
        __builtin_amdgcn_raw_buffer_store_b128((u32x4){t0[0], t1[0], t0[1], t1[1]}, rsO, ooff + 1024, 0, 16);
        if (wid >= 4) {
            if (lane == 0) __hip_atomic_fetch_add(sync + 1, 1u, __ATOMIC_RELAXED, __HIP_MEMORY_SCOPE_WORKGROUP);
            return;
        }
    }
    const int bn = obn, kq = wid;
    bf16x8 wf[16][2];
    {
        const int d0 = (kq + (r >> 4)) & 3, d1 = (kq + 2 + (r >> 4)) & 3;
        const char* Wp = ws + WS_WO + ((size_t)(kq * 16) * 16 + bn * 2) * 1024 + (h * 32 + (r & 15)) * 16;
        const char* Wp0 = Wp + (d0 >> 1) * 1024 + (d0 & 1) * 256;
        const char* Wp1 = Wp + (d1 >> 1) * 1024 + (d1 & 1) * 256;
#pragma unroll
        for (int k = 0; k < 16; ++k) { wf[k][0] = *(const bf16x8*)(Wp0 + k * 16384); wf[k][1] = *(const bf16x8*)(Wp1 + k * 16384); }
    }
    {
        gu32* cnt = (gu32*)(ws + WS_CNT + (qb * 8 + ort) * 256);
#pragma unroll 1
        for (unsigned spins = 0; spins < (1u << 19); ++spins) {
            if ((unsigned)__builtin_amdgcn_readfirstlane(__hip_atomic_load(cnt, __ATOMIC_RELAXED, __HIP_MEMORY_SCOPE_AGENT)) >= 64u) break;
            __builtin_amdgcn_s_sleep(8);
        }
        asm volatile("" ::: "memory");
    }
    f32x4* red = (f32x4*)(smem + A_RED);
#pragma unroll 1
    for (int ph = 0; ph < 2; ++ph) {
        const int row0 = qb * 512 + ph * 256 + ort * 32;
        const unsigned aoff = (unsigned)(kq * 16 * SEQ + row0) * 32 + lane * 16;
        if (ph == 1) {
#pragma unroll 1
            for (unsigned spins = 0; spins < (1u << 21); ++spins) { if (lds_ld(sync + 1) >= 4u) break; __builtin_amdgcn_s_sleep(4); }
            __builtin_amdgcn_s_sleep(LATE_DELAY);
        }
        f32x16 acc[2];
#pragma unroll 1
        for (unsigned spins = 0; spins < (1u << 18); ++spins) {
            asm volatile("" ::: "memory");
            u32x4 of[16];
#pragma unroll
            for (int k = 0; k < 16; ++k) of[k] = __builtin_amdgcn_raw_buffer_load_b128(rsO, aoff, k * SEQ * 32, 16);
            SBAR();
            acc[0] = (f32x16){}; acc[1] = (f32x16){};
            unsigned mx = 0;
#pragma unroll
            for (int k = 0; k < 16; ++k) {
                acc[0] = MFMA32(wf[k][0], __builtin_bit_cast(bf16x8, of[k]), acc[0]);
                acc[1] = MFMA32(wf[k][1], __builtin_bit_cast(bf16x8, of[k]), acc[1]);
                mx = max(max(mx, max(of[k][0], of[k][1])), max(of[k][2], of[k][3]));
                PIN(mx); SBAR();
            }
            if (!__any(mx == ~0u)) break;
            __builtin_amdgcn_s_sleep(POLL_SLEEP);
        }
        f32x4 own[2];
#pragma unroll
        for (int j = 0; j < 4; ++j)
#pragma unroll
            for (int g = 0; g < 2; ++g) {
                const int e = 4 * (2 * (j & 1) + g);
                const f32x4 v = {acc[j >> 1][e], acc[j >> 1][e + 1], acc[j >> 1][e + 2], acc[j >> 1][e + 3]};
                if (j == 0) own[g] = v;
                else red[((((kq + j) & 3) * 3 + j - 1) * 2 + g) * 64 + lane] = v;
            }
        if (ph == 0) lds_arrive_wait(sync + 4, 4u, lane);
        else LBAR();
#pragma unroll
        for (int sl = 0; sl < 3; ++sl)
#pragma unroll
            for (int g = 0; g < 2; ++g) own[g] += red[((kq * 3 + sl) * 2 + g) * 64 + lane];
        auto s0 = __builtin_amdgcn_permlane32_swap(cvtpk(own[0][0], own[0][1]), cvtpk(own[1][0], own[1][1]), false, false);
        auto s1 = __builtin_amdgcn_permlane32_swap(cvtpk(own[0][2], own[0][3]), cvtpk(own[1][2], own[1][3]), false, false);
        *(u32x4*)(out + (size_t)(row0 + r) * DM + bn * 64 + kq * 16 + h * 8) = (u32x4){s0[0], s1[0], s0[1], s1[1]};
        if (ph == 0) { lds_arrive_wait(sync + 5, 4u, lane); }
    }
}

extern "C" void kernel_launch(void* const* d_in, const int* in_sizes, int n_in,
                              void* d_out, int out_size, void* d_ws, size_t ws_size,
                              hipStream_t stream) {
    const u16* x = (const u16*)d_in[0];
    const u16* wq = (const u16*)d_in[1];
    const u16* wk = (const u16*)d_in[2];
    const u16* wv = (const u16*)d_in[3];
    const u16* woA = (const u16*)d_in[4];
    const u16* wqkv = (const u16*)d_in[6];
    const u16* woB = (const u16*)d_in[7];
    const u16* gw = (const u16*)d_in[8];
    char* ws = (char*)d_ws;
    (void)hipFuncSetAttribute((const void*)k_proj, hipFuncAttributeMaxDynamicSharedMemorySize, P_LDS);
    (void)hipFuncSetAttribute((const void*)k_attn, hipFuncAttributeMaxDynamicSharedMemorySize, A_LDS);
    k_proj<<<256, 640, P_LDS, stream>>>(x, wq, wk, wv, wqkv, woA, woB, gw, ws);
    k_attn<<<256, 512, A_LDS, stream>>>(ws, (u16*)d_out);
}
```
